```python
import torch

def _softmax(x, axis=-1):
    return torch.softmax(x, dim=axis)

def _gelu(x):
    return torch.nn.functional.gelu(x, approximate='tanh')

def _silu(x):
    return torch.nn.functional.silu(x)

def _sigmoid(x):
    return torch.sigmoid(x)

def _layernorm(x, axis=-1, eps=1e-05):
    m = x.mean(dim=axis, keepdim=True)
    v = x.var(dim=axis, keepdim=True, unbiased=False)
    return (x - m) / torch.sqrt(v + eps)

def _scatter_add(tgt, idx, src, axis=0):
    return tgt.clone().index_add_(axis, idx, src)

def _segment_sum(src, seg, n):
    out = torch.zeros((n,) + src.shape[1:], dtype=src.dtype)
    return out.index_add_(0, seg, src)

def _pad(x, pw):
    flat = tuple((v for pair in reversed(pw) for v in pair))
    return torch.nn.functional.pad(x, flat)
kernel_name = 'adaLN_cond__mha_block_cached__swiglu_ffn__decode'

def setup_inputs():

    _w = lambda a: (lambda t: t.to(torch.bfloat16) if t.is_floating_point() else t)(torch.as_tensor(a))

    def _A_setup():
        B, Sq, D = (1, 1, 576)
        H, Sk, Dh = (32, 8192, 128)
        return {'x': _w((lambda *s: torch.randn(*s, dtype=torch.bfloat16) * 0.05)(B, Sq, D)), 'k_cache': _w((lambda *s: torch.randn(*s, dtype=torch.bfloat16) * 3.2)(B, H, Sk, Dh)), 'v_cache': _w((lambda *s: torch.randn(*s, dtype=torch.bfloat16) * 0.4)(B, H, Sk, Dh)), 'wq': _w((lambda *s: torch.randn(*s, dtype=torch.bfloat16) * 3.2)(D, H * Dh) * D ** (-0.5)), 'wo': _w((lambda *s: torch.randn(*s, dtype=torch.bfloat16) * 0.4)(H * Dh, D) * (H * Dh) ** (-0.5))}
    _A_in = _A_setup()

    def _B_setup():
        B, S, D, F = (1, 1, 576, 4608)
        return {'x': _w((lambda *s: torch.randn(*s, dtype=torch.bfloat16) * 0.05)(B, S, D)), 'w_gate': _w((lambda *s: torch.randn(*s, dtype=torch.bfloat16) * 0.05)(D, F)), 'w_up': _w((lambda *s: torch.randn(*s, dtype=torch.bfloat16) * 0.05)(D, F)), 'w_down': _w((lambda *s: torch.randn(*s, dtype=torch.bfloat16) * 0.05)(F, D))}
    _B_in = _B_setup()
    return {'A_x': _A_in['x'], 'A_k_cache': _A_in['k_cache'], 'A_v_cache': _A_in['v_cache'], 'A_wq': _A_in['wq'], 'A_wo': _A_in['wo'], 'B_x': _B_in['x'], 'B_w_gate': _B_in['w_gate'], 'B_w_up': _B_in['w_up'], 'B_w_down': _B_in['w_down'], 'c': _w((lambda *s: torch.randn(*s, dtype=torch.bfloat16) * 0.1)(1, 576) * 8.0), 'w_mod': _w((lambda *s: torch.randn(*s, dtype=torch.bfloat16) * 0.1)(576, 6 * 576) * 576 ** (-0.5) * 8.0)}

def reference(A_x, A_k_cache, A_v_cache, A_wq, A_wo, B_x, B_w_gate, B_w_up, B_w_down, c, w_mod):

    def _A_ref(x, k_cache, v_cache, wq, wo):
        B, Sq, D = x.shape
        H, Dh = (32, 128)
        q = (lambda x, *d: x.permute(*d))((x @ wq).reshape(B, Sq, H, Dh), 0, 2, 1, 3)
        scores = torch.einsum('bhqd,bhkd->bhqk', q, k_cache) * Dh ** (-0.5)
        p = _softmax(scores, axis=-1)
        o = torch.einsum('bhqk,bhkd->bhqd', p, v_cache)
        return (lambda x, *d: x.permute(*d))(o, 0, 2, 1, 3).reshape(B, Sq, H * Dh) @ wo

    def _B_ref(x, w_gate, w_up, w_down):
        g = _silu(x @ w_gate)
        u = x @ w_up
        return g * u @ w_down
    x = A_x
    mod = (_silu(c) @ w_mod).reshape(c.shape[0], 6, 1, -1)
    sa, ca, ga, sb, cb, gb = (mod[:, 0].clone(), mod[:, 1].clone(), mod[:, 2].clone(), mod[:, 3].clone(), mod[:, 4].clone(), mod[:, 5].clone())
    h = _layernorm(x, axis=-1) * (1 + ca) + sa
    x = x + ga * _A_ref(h, A_k_cache, A_v_cache, A_wq, A_wo)
    h = _layernorm(x, axis=-1) * (1 + cb) + sb
    x = x + gb * _B_ref(h, B_w_gate, B_w_up, B_w_down)
    return x
```

```cpp
#include <hip/hip_runtime.h>
#include <hip/hip_bf16.h>
#include <stdint.h>

#define EMU 0

typedef unsigned short u16;
__device__ __forceinline__ float bf2f(u16 b) { return __uint_as_float(((unsigned)b) << 16); }
__device__ __forceinline__ u16 f2bf(float f) { __hip_bfloat16 h = __float2bfloat16(f); return *(u16*)&h; }
__device__ __forceinline__ float rb(float f) {
  return f;
}
constexpr int D = 576, H = 32, SK = 8192, DH = 128, F = 4608, NSPLIT = 8;

__device__ float block_sum(float v, float* red) {
  for (int o = 32; o > 0; o >>= 1) v += __shfl_xor(v, o);
  int w = threadIdx.x >> 6;
  __syncthreads();
  if ((threadIdx.x & 63) == 0) red[w] = v;
  __syncthreads();
  float s = 0;
  for (int i = 0; i < (int)(blockDim.x >> 6); ++i) s += red[i];
  return s;
}

__global__ void k_mod(const u16* c, const u16* wmod, float* mod) {
  __shared__ float sc[D];
  for (int i = threadIdx.x; i < D; i += blockDim.x) { float v = bf2f(c[i]); sc[i] = rb(v / (1.f + __expf(-v))); }
  __syncthreads();
  int n = blockIdx.x * blockDim.x + threadIdx.x;
  if (n >= 6 * D) return;
  float a = 0;
  for (int k = 0; k < D; ++k) a += sc[k] * bf2f(wmod[(size_t)k * 6 * D + n]);
  mod[n] = rb(a);
}

__device__ void ln_mod(const float* xv  , const float* sh, const float* sc, float* hout, float* red) {
  float s = 0;
  for (int i = threadIdx.x; i < D; i += blockDim.x) s += xv[i];
  float mean = block_sum(s, red) / D;
  float q = 0;
  for (int i = threadIdx.x; i < D; i += blockDim.x) { float d = xv[i] - mean; q += d * d; }
  float var = block_sum(q, red) / D;
  float m = rb(mean), v = rb(var);
  float den = rb(sqrtf(rb(v + 1e-5f)));
  for (int i = threadIdx.x; i < D; i += blockDim.x) {
    float ln = rb(rb(xv[i] - m) / den);
    hout[i] = rb(rb(ln * rb(1.f + sc[i])) + sh[i]);
  }
  __syncthreads();
}

__global__ void k_q(const u16* x, const float* mod, const u16* wq, float* q) {
  __shared__ float xv[D], h[D], red[16];
  for (int i = threadIdx.x; i < D; i += blockDim.x) xv[i] = bf2f(x[i]);
  __syncthreads();
  ln_mod(xv, mod, mod + D, h, red);
  int n = blockIdx.x * blockDim.x + threadIdx.x;
  float a = 0;
  for (int k = 0; k < D; ++k) a += h[k] * bf2f(wq[(size_t)k * H * DH + n]);
  q[n] = rb(a);
}

__global__ void k_attn(const float* q, const u16* kc, const u16* vc, float* po, float* pml) {
  __shared__ float sq[DH], sc[SK / NSPLIT], red[16];
  int h = blockIdx.x / NSPLIT, s = blockIdx.x % NSPLIT;
  const int NK = SK / NSPLIT;
  if (threadIdx.x < DH) sq[threadIdx.x] = q[h * DH + threadIdx.x];
  __syncthreads();
  float mx = -INFINITY;
  for (int j = threadIdx.x; j < NK; j += blockDim.x) {
    const u16* kr = kc + ((size_t)h * SK + s * NK + j) * DH;
    float a = 0;
    for (int d = 0; d < DH; ++d) a += sq[d] * bf2f(kr[d]);
    a = rb(rb(a) * 0.08838834764831845f);
    sc[j] = a;
    mx = fmaxf(mx, a);
  }
  for (int o = 32; o > 0; o >>= 1) mx = fmaxf(mx, __shfl_xor(mx, o));
  __syncthreads();
  if ((threadIdx.x & 63) == 0) red[threadIdx.x >> 6] = mx;
  __syncthreads();
  mx = -INFINITY;
  for (int i = 0; i < (int)(blockDim.x >> 6); ++i) mx = fmaxf(mx, red[i]);
  float l = 0;
  for (int j = threadIdx.x; j < NK; j += blockDim.x) { float p = __expf(sc[j] - mx); sc[j] = p; l += p; }
  l = block_sum(l, red);
  if (threadIdx.x < DH) {
    float a = 0;
    for (int j = 0; j < NK; ++j) a += sc[j] * bf2f(vc[((size_t)h * SK + s * NK + j) * DH + threadIdx.x]);
    po[blockIdx.x * DH + threadIdx.x] = a;
  }
  if (threadIdx.x == 0) { pml[blockIdx.x * 2] = mx; pml[blockIdx.x * 2 + 1] = l; }
}

__global__ void k_wo(const float* po, const float* pml, const u16* wo, const u16* x, const float* mod, float* x2) {
  __shared__ float o[H * DH];
  for (int i = threadIdx.x; i < H * DH; i += blockDim.x) {
    int h = i / DH, d = i % DH;
    float M = -INFINITY;
    for (int s = 0; s < NSPLIT; ++s) M = fmaxf(M, pml[(h * NSPLIT + s) * 2]);
    float L = 0, a = 0;
    for (int s = 0; s < NSPLIT; ++s) { float w = __expf(pml[(h * NSPLIT + s) * 2] - M); L += w * pml[(h * NSPLIT + s) * 2 + 1]; a += w * po[(h * NSPLIT + s) * DH + d]; }
    o[i] = rb(a / L);
  }
  __syncthreads();
  int n = blockIdx.x * blockDim.x + threadIdx.x;
  if (n >= D) return;
  float a = 0;
  for (int k = 0; k < H * DH; ++k) a += o[k] * bf2f(wo[(size_t)k * D + n]);
  a = rb(a);
  x2[n] = rb(bf2f(x[n]) + rb(mod[2 * D + n] * a));
}

__global__ void k_gu(const float* x2, const float* mod, const u16* wg, const u16* wu, float* gu) {
  __shared__ float xv[D], h[D], red[16];
  for (int i = threadIdx.x; i < D; i += blockDim.x) xv[i] = x2[i];
  __syncthreads();
  ln_mod(xv, mod + 3 * D, mod + 4 * D, h, red);
  int n = blockIdx.x * blockDim.x + threadIdx.x;
  float g = 0, u = 0;
  for (int k = 0; k < D; ++k) { g += h[k] * bf2f(wg[(size_t)k * F + n]); u += h[k] * bf2f(wu[(size_t)k * F + n]); }
  g = rb(g); u = rb(u);
  g = rb(g / (1.f + __expf(-g)));
  gu[n] = rb(g * u);
}

__global__ void k_down(const float* gu, const u16* wd, const float* x2, const float* mod, u16* out) {
  __shared__ float s[F];
  for (int i = threadIdx.x; i < F; i += blockDim.x) s[i] = gu[i];
  __syncthreads();
  int n = blockIdx.x * blockDim.x + threadIdx.x;
  if (n >= D) return;
  float a = 0;
  for (int k = 0; k < F; ++k) a += s[k] * bf2f(wd[(size_t)k * D + n]);
  a = rb(a);
  out[n] = f2bf(x2[n] + rb(mod[5 * D + n] * a));
}

extern "C" void kernel_launch(void* const* d_in, const int* in_sizes, int n_in, void* d_out, int out_size, void* d_ws, size_t ws_size, hipStream_t stream) {
  const u16* x = (const u16*)d_in[0];
  const u16* kc = (const u16*)d_in[1];
  const u16* vc = (const u16*)d_in[2];
  const u16* wq = (const u16*)d_in[3];
  const u16* wo = (const u16*)d_in[4];
  const u16* wg = (const u16*)d_in[6];
  const u16* wu = (const u16*)d_in[7];
  const u16* wd = (const u16*)d_in[8];
  const u16* c = (const u16*)d_in[9];
  const u16* wmod = (const u16*)d_in[10];
  float* ws = (float*)d_ws;
  float *mod = ws, *q = ws + 4096, *po = ws + 8192, *pml = po + 256 * 128, *x2 = pml + 512, *gu = x2 + 1024;
  k_mod<<<(6 * D + 63) / 64, 64, 0, stream>>>(c, wmod, mod);
  k_q<<<H * DH / 64, 64, 0, stream>>>(x, mod, wq, q);
  k_attn<<<H * NSPLIT, 256, 0, stream>>>(q, kc, vc, po, pml);
  k_wo<<<D / 64, 64, 0, stream>>>(po, pml, wo, x, mod, x2);
  k_gu<<<F / 64, 64, 0, stream>>>(x2, mod, wg, wu, gu);
  k_down<<<D / 64, 64, 0, stream>>>(gu, wd, x2, mod, (u16*)d_out);
}
```

```cpp
#include <hip/hip_runtime.h>
#include <stdint.h>

#define STAMPS 0
#define WK 4
#define KQ 24
#define AUX_SH 0
#define NT_GU 1
#define Z_WO 0
#define Z_GU 0
#define Z_PRE 0
#define AUX_WD 2

typedef unsigned u32;
typedef unsigned long long u64;
typedef unsigned short u16;
typedef u32 u2 __attribute__((ext_vector_type(2)));
typedef u32 u3 __attribute__((ext_vector_type(3)));
typedef u32 u3a __attribute__((ext_vector_type(3), aligned(4)));
typedef u32 u4 __attribute__((ext_vector_type(4)));
typedef float f2 __attribute__((ext_vector_type(2)));
typedef float f4 __attribute__((ext_vector_type(4)));
typedef __bf16 bf2 __attribute__((ext_vector_type(2)));
typedef __bf16 bf8 __attribute__((ext_vector_type(8)));
typedef __amdgpu_buffer_rsrc_t rsrc_t;
#define LAS __attribute__((address_space(3)))
#define GAS __attribute__((address_space(1)))
#define DI __device__ __forceinline__

constexpr int D = 576, FF = 4608;
constexpr u32 SENT = 0xFFFFFFFFu;
constexpr int SC1 = 16, NT = 2;
constexpr u32 SPIN_G = 1u << 17, SPIN_L = 1u << 20;

constexpr int O_EPOCH = 0;
constexpr int O_RDY = 4096;
constexpr int O_HG = 8192;
constexpr int HG_STR = 5120;
constexpr int O_QG = 65536;
constexpr int O_MODL = 98304 + 0;
constexpr int O_ML = 106496;
constexpr int O_X2 = 122880;
constexpr int X2_STR = 1280;
constexpr int O_YA = 262144;
constexpr int O_FC = O_YA + 48 * 16384;
constexpr int O_END = O_FC + 36 * 12288;
constexpr int O_STAMP = 2 << 20;

DI float asf(u32 x) { return __uint_as_float(x); }
DI u32 asu(float x) { return __float_as_uint(x); }
DI u32 pk(float lo, float hi) { f2 t = {lo, hi}; bf2 r = __builtin_convertvector(t, bf2); return __builtin_bit_cast(u32, r); }
DI float rb(float x) { return asf(pk(0.f, x)); }
DI float lo16(u32 x) { return asf(x << 16); }
DI float hi16(u32 x) { return asf(x & 0xffff0000u); }
template <int C> DI u32 dppu(u32 x) { return (u32)__builtin_amdgcn_update_dpp(0, (int)x, C, 0xf, 0xf, true); }
template <int C> DI float dpp(float x) { return asf(dppu<C>(asu(x))); }
constexpr int ROR4 = 0x124, ROR8 = 0x128, HMIR = 0x141, QX1 = 0xB1, QX2 = 0x4E, SHL = 0x100, NBC = 0x150;
DI float dot2(u32 a, u32 b, float c) { return __builtin_amdgcn_fdot2_f32_bf16(__builtin_bit_cast(bf2, a), __builtin_bit_cast(bf2, b), c, false); }
DI float sw32(float a, float b) { auto r = __builtin_amdgcn_permlane32_swap(asu(a), asu(b), false, false); u32 r0 = r[0], r1 = r[1]; return asf(r0) + asf(r1); }
DI float sw16(float a, float b) { auto r = __builtin_amdgcn_permlane16_swap(asu(a), asu(b), false, false); u32 r0 = r[0], r1 = r[1]; return asf(r0) + asf(r1); }
DI float mx32(float a) { auto r = __builtin_amdgcn_permlane32_swap(asu(a), asu(a), false, false); u32 r0 = r[0], r1 = r[1]; return fmaxf(asf(r0), asf(r1)); }
DI float mx16(float a) { auto r = __builtin_amdgcn_permlane16_swap(asu(a), asu(a), false, false); u32 r0 = r[0], r1 = r[1]; return fmaxf(asf(r0), asf(r1)); }
DI float wave_sum(float v) {
  v += dpp<QX1>(v); v += dpp<QX2>(v); v += dpp<ROR4>(v); v += dpp<ROR8>(v);
  v = sw16(v, v); return sw32(v, v);
}
DI float wave_max(float v) {
  v = fmaxf(v, dpp<QX1>(v)); v = fmaxf(v, dpp<QX2>(v)); v = fmaxf(v, dpp<ROR4>(v)); v = fmaxf(v, dpp<ROR8>(v));
  return mx32(mx16(v));
}
DI u32 plo(u32 a, u32 b) { return __builtin_amdgcn_perm(b, a, 0x05040100u); }
DI u32 phi(u32 a, u32 b) { return __builtin_amdgcn_perm(b, a, 0x07060302u); }
DI u32 canon(u32 x) { return x == SENT ? 0x7FC00000u : x; }
DI f4 mfma(u4 a, u4 b, f4 c) { return __builtin_amdgcn_mfma_f32_16x16x32_bf16(__builtin_bit_cast(bf8, a), __builtin_bit_cast(bf8, b), c, 0, 0, 0); }
DI rsrc_t mkrs(const void* p, u32 n) { return __builtin_amdgcn_make_buffer_rsrc((void*)p, 0, n, 0x00020000); }
#define FENCE() asm volatile("" ::: "memory")
#define SB() __builtin_amdgcn_sched_barrier(0)
#define PIN(v) asm volatile("" : "+v"(v))
#define VMCNT(n) asm volatile("s_waitcnt vmcnt(%0)" ::"n"(n) : "memory")
DI void bar() { asm volatile("s_waitcnt lgkmcnt(0)\n\ts_barrier" ::: "memory"); }
DI float silu(float v) { return v * __builtin_amdgcn_rcpf(1.f + __expf(-v)); }

struct __attribute__((aligned(16))) Sh {
  float op[8][128];
  float gp[48][20];
  float red[16][12];
  float red2[16][12];
  float mp[2][3][16];
  float st[12][2];
  u32 q[64];
  u32 ob[64];
  u32 sc[576];
  u2 ml[8];
  u2 gml[2];
  u32 flag[8];
};

#define STAMP(i)
#define STAMP_V(i, v)
#define STAMP_OUT()

DI void lds_wait(volatile LAS u32* f) {
  for (u32 n = 0; n < SPIN_L; ++n) { if (*f != 0) break; __builtin_amdgcn_s_sleep(1); }
  FENCE();
}

DI float tred16(const float* s, int lane) {
  float t[8], u[4], v[2];
  const bool b3 = lane & 8, b2 = lane & 4, b1 = lane & 2, b0 = lane & 1;
#pragma unroll
  for (int i = 0; i < 8; ++i) t[i] = (b3 ? s[i + 8] : s[i]) + dpp<ROR8>(b3 ? s[i] : s[i + 8]);
#pragma unroll
  for (int i = 0; i < 4; ++i) u[i] = (b2 ? t[i + 4] : t[i]) + dpp<HMIR>(b2 ? t[i] : t[i + 4]);
#pragma unroll
  for (int i = 0; i < 2; ++i) v[i] = (b1 ? u[i + 2] : u[i]) + dpp<QX2>(b1 ? u[i] : u[i + 2]);
  return (b0 ? v[1] : v[0]) + dpp<QX1>(b0 ? v[0] : v[1]);
}

template <int C, typename V> DI void frag(const V* L, u4* F) {
#pragma unroll
  for (int c = 0; c < C; ++c) {
    u4 a, b;
#pragma unroll
    for (int j = 0; j < 4; ++j) { const u32 x = L[2 * j][c], y = L[2 * j + 1][c]; a[j] = plo(x, y); b[j] = phi(x, y); }
    F[2 * c] = a; F[2 * c + 1] = b;
  }
}

DI u4 gather8(float v) {
  const u32 d = pk(v, dpp<SHL + 1>(v));
  u4 a; a[0] = d; a[1] = dppu<SHL + 2>(d); a[2] = dppu<SHL + 4>(d); a[3] = dppu<SHL + 6>(d);
  return a;
}

DI void job_dot(const u4* W, const u32* act, LAS float* dst, int lane) {
  float a[8];
#pragma unroll
  for (int c = 0; c < 4; ++c) {
    float e = 0.f, o = 0.f;
#pragma unroll
    for (int p = 0; p < 3; ++p) { const u32 x = W[2 * p][c], y = W[2 * p + 1][c]; e = dot2(plo(x, y), act[p], e); o = dot2(phi(x, y), act[p], o); }
    a[2 * c] = e; a[2 * c + 1] = o;
  }
  float b[4], r[2];
#pragma unroll
  for (int i = 0; i < 4; ++i) b[i] = sw32(a[i], a[i + 4]);
#pragma unroll
  for (int i = 0; i < 2; ++i) { float v = sw16(b[i], b[i + 2]); v += dpp<ROR8>(v); v += dpp<ROR4>(v); v += dpp<QX2>(v); r[i] = v; }
  if ((lane & 14) == 0) { f2 w = {r[0], r[1]}; *(LAS f2*)(dst + 8 * (lane & 1) + 2 * (lane >> 4)) = w; }
}

struct Ctx {
  const u16 *x, *wg, *wu, *wd; u16* out; char* ws;
  int b, xc, s, hl, h, lane, wave;
};

template <bool STREAM>
DI void tail(const Ctx& c, LAS Sh* sh, rsrc_t rws, const u4* WF, u3* gw, const char* gup, u32 gstr
) {
  const int lane = c.lane, wave = c.wave;
  char* ws = c.ws; (void)ws;
  bar();
  if (!STREAM && wave < 10) {
    const int d = (wave - 8) * 64 + lane;
    float v[8];
#pragma unroll
    for (int w = 0; w < 8; ++w) v[w] = sh->op[w][d];
    const float o = ((v[0] + v[1]) + (v[2] + v[3])) + ((v[4] + v[5]) + (v[6] + v[7]));
    ((LAS u16*)sh->ob)[d] = (u16)pk(o, 0.f);
  }
  bar();
  STAMP(8);
  {
    const LAS u32* ob = sh->ob + 8 * (lane >> 4) + 4 * (lane & 1);
    const u4 a0 = *(const LAS u4*)ob, a1 = *(const LAS u4*)(ob + 32);
    f4 dd[6];
#pragma unroll
    for (int j = 0; j < 6; ++j) { f4 z = {0.f, 0.f, 0.f, 0.f}; dd[j] = mfma(a0, WF[j], z); }
#pragma unroll
    for (int j = 0; j < 6; ++j) dd[j] = mfma(a1, WF[6 + j], dd[j]);
    const bool half = lane & 8;
    u4 o;
#pragma unroll
    for (int i = 0; i < 3; ++i) {
      const float own = half ? dd[3 + i][1] : dd[i][0], snd = half ? dd[i][1] : dd[3 + i][0];
      o[i] = canon(asu(own + dpp<ROR8>(snd)));
    }
    o[3] = 0;
    const int ch = 16 * wave + 2 * (lane & 7) + ((lane >> 3) & 1);
    if (lane < 16) __builtin_amdgcn_raw_buffer_store_b128(o, rws, O_YA + (ch >> 2) * 16384 + c.b * 64 + (ch & 3) * 16, 0, SC1);
  }
  STAMP(9);
  if (c.s < 6) {
    const int fl = c.hl * 6 + c.s;
    if (STREAM || wave == 8) {
      if (!STREAM) {
#pragma unroll
        for (int j = 0; j < 8; ++j) gw[j] = NT_GU ? __builtin_nontemporal_load((const GAS u3a*)(gup + j * gstr)) : *(const GAS u3a*)(gup + j * gstr);
      }
      const int xo = O_X2 + c.xc * X2_STR + (64 * wave + lane) * 2;
      u32 xr = __builtin_amdgcn_raw_buffer_load_b16(rws, xo, 0, SC1);
      SB();
      u4 GF[6];
      frag<3>(gw, GF);
#pragma unroll
      for (int j = 0; j < 6; ++j) PIN(GF[j]);
      const u32 scv = sh->sc[64 * wave + lane];
      SB();
      STAMP_V(10, GF[0]);
      for (u32 n = 0; n < SPIN_G; ++n) {
        if (__all(xr != 0xFFFFu)) break;
        FENCE();
        xr = __builtin_amdgcn_raw_buffer_load_b16(rws, xo, 0, SC1);
      }
      STAMP(11);
      const float xv = lo16(xr);
      {
        const float s1 = wave_sum(xv);
        const float dm = xv - s1 * (1.f / 64.f);
        const float s2 = wave_sum(dm * dm);
        if (lane == 0) { f2 w = {s1, s2}; *(LAS f2*)sh->st[wave] = w; }
      }
      bar();
      float mean, var;
      {
        f4 t[5];
#pragma unroll
        for (int i = 0; i < 5; ++i) t[i] = *(const LAS f4*)sh->st[2 * i];
        mean = ((t[0][0] + t[0][2]) + (t[1][0] + t[1][2]) + ((t[2][0] + t[2][2]) + (t[3][0] + t[3][2])) + t[4][0]) * (1.f / D);
        float m2 = ((t[0][1] + t[0][3]) + (t[1][1] + t[1][3]) + ((t[2][1] + t[2][3]) + (t[3][1] + t[3][3])) + t[4][1]);
        float e = 0.f;
#pragma unroll
        for (int i = 0; i < 9; ++i) { const float dl = t[i / 2][(i & 1) * 2] * (1.f / 64.f) - mean; e = fmaf(dl, dl, e); }
        var = (m2 + 64.f * e) * (1.f / D);
      }
      const float den = rb(__builtin_sqrtf(rb(rb(var) + 1e-5f)));
      const float ln = rb(rb(xv - rb(mean)) * __builtin_amdgcn_rcpf(den));
      const float h2 = rb(ln * hi16(scv)) + lo16(scv);
      const u4 a = gather8(h2);
      f4 dd[6];
#pragma unroll
      for (int j = 0; j < 6; ++j) { f4 z = {0.f, 0.f, 0.f, 0.f}; dd[j] = mfma(a, GF[j], z); }
      const int par = (lane >> 2) & 1;
      if ((lane >> 4) == 2 * par) {
        LAS float* gp = &sh->gp[24 * ((lane >> 3) & 1) + 6 * (lane & 3)][2 * wave + par];
#pragma unroll
        for (int j = 0; j < 6; ++j) gp[20 * j] = dd[j][0];
      }
      bar();
      STAMP(12);
    } else if (!STREAM) {
      const int dw = wave - 9, g = lane >> 4;
      const rsrc_t rwd = mkrs(c.wd + (size_t)(576 * c.xc + 24 * fl) * D, 24 * D * 2);
      u2 wd[3][8];
      const int vo = 8 * g * (D * 2) + (192 * dw + 4 * (lane & 15)) * 2;
#pragma unroll
      for (int j = 0; j < 8; ++j)
#pragma unroll
        for (int e = 0; e < 3; ++e) wd[e][j] = __builtin_amdgcn_raw_buffer_load_b64(rwd, vo + e * 128, j * (D * 2), AUX_WD);
      u4 DF[12];
#pragma unroll
      for (int e = 0; e < 3; ++e) frag<2>(wd[e], DF + 4 * e);
#pragma unroll
      for (int j = 0; j < 12; ++j) PIN(DF[j]);
      SB();
      STAMP_V(10, DF[0]);
      bar();
      bar();
      STAMP(12);
      const int gg = g < 3 ? g : 0;
      const LAS float* gp = sh->gp[24 * ((lane >> 3) & 1) + 8 * gg + (lane & 7)];
      f4 t[5];
#pragma unroll
      for (int i = 0; i < 5; ++i) t[i] = *(const LAS f4*)(gp + 4 * i);
      const f4 tt = (t[0] + t[1]) + (t[2] + t[3]) + t[4];
      const float gs = rb((tt[0] + tt[1]) + (tt[2] + tt[3]));
      const float us = dpp<SHL + 8>(gs);
      float gu = rb(silu(gs)) * us;
      gu = g < 3 ? gu : 0.f;
      const u4 a = gather8(gu);
      const int fp = c.xc * 24 + fl;
#pragma unroll
      for (int e = 0; e < 3; ++e) {
        u4 o;
#pragma unroll
        for (int j = 0; j < 4; ++j) { f4 z = {0.f, 0.f, 0.f, 0.f}; const f4 r = mfma(a, DF[4 * e + j], z); o[j] = canon(asu(r[0])); }
        const int ch = 48 * dw + 16 * e + (lane & 15);
        if (lane < 16) __builtin_amdgcn_raw_buffer_store_b128(o, rws, O_FC + (ch >> 2) * 12288 + fp * 64 + (ch & 3) * 16, 0, SC1);
      }
      STAMP(13);
    }
  } else {
    const int rho = c.xc + 8 * (c.hl * 2 + (c.s - 6));
    if (rho < 48) {
      float xin = 0.f; u32 gv = 0;
      if (STREAM) {
        const int col = 12 * rho + ((lane & 15) < 12 ? (lane & 15) : 0);
        if (wave < 2) { xin = lo16(c.x[col]); gv = sh->sc[col]; }
        const int o = O_YA + rho * 16384 + wave * 2048 + lane * 16;
        u4 v0, v1;
        for (u32 n = 0; n < SPIN_G; ++n) {
          FENCE();
          v0 = __builtin_amdgcn_raw_buffer_load_b128(rws, o, 0, SC1);
          v1 = __builtin_amdgcn_raw_buffer_load_b128(rws, o + 1024, 0, SC1);
          const bool ok = v0[0] != SENT && v0[1] != SENT && v0[2] != SENT && v1[0] != SENT && v1[1] != SENT && v1[2] != SENT;
          if (__all(ok)) break;
        }
        STAMP(10);
        float y[3];
#pragma unroll
        for (int i = 0; i < 3; ++i) { const u32 p = v0[i], q = v1[i]; float t = asf(p) + asf(q); t += dpp<ROR4>(t); t += dpp<ROR8>(t); y[i] = t; }
        const float r = sw16(sw32(y[0], y[2]), sw32(y[1], y[1]));
        if ((lane & 12) == 0 && lane < 48) sh->red[3 * (lane & 3) + (lane >> 4)][wave] = r;
      }
      bar();
      if (STREAM && wave < 2) {
        const LAS float* rp = sh->red[(lane & 15) < 12 ? (lane & 15) : 0];
        const f4 t0 = *(const LAS f4*)rp, t1 = *(const LAS f4*)(rp + 4);
        const f4 tt = t0 + t1;
        const float y = rb((tt[0] + tt[1]) + (tt[2] + tt[3]));
        const float x2 = xin + rb(lo16(gv) * y);
        u32 d = pk(x2, dpp<SHL + 1>(x2));
        d = (d & 0xFFFFu) == 0xFFFFu ? (d & 0xFFFF7FFFu) : d;
        d = (d >> 16) == 0xFFFFu ? (d & 0x7FFFFFFFu) : d;
        u3 o; o[0] = d; o[1] = dppu<SHL + 2>(d); o[2] = dppu<SHL + 4>(d);
        const int l16 = lane & 15;
        if (l16 == 0 || l16 == 6) __builtin_amdgcn_raw_buffer_store_b96(o, rws, O_X2 + (4 * wave + (lane >> 4)) * X2_STR + (12 * rho + l16) * 2, 0, SC1);
        STAMP(11);
      }
    }
    if (rho >= 28) {
      const int r2 = rho - 28;
      u32 xr = 0, gv = 0;
      if (STREAM && wave == 0) {
        const int xo = O_X2 + c.xc * X2_STR + (16 * r2 + (lane & 15)) * 2;
        for (u32 n = 0; n < SPIN_G; ++n) {
          FENCE();
          xr = __builtin_amdgcn_raw_buffer_load_b16(rws, xo, 0, SC1);
          if (__all(xr != 0xFFFFu)) break;
        }
        gv = sh->sc[16 * r2 + (lane & 15)];
      }
      const int o = O_FC + r2 * 12288 + wave * 1024 + lane * 16;
      u4 v;
      for (u32 n = 0; n < SPIN_G; ++n) {
        FENCE();
        v = __builtin_amdgcn_raw_buffer_load_b128(rws, o, 0, SC1);
        const bool ok = v[0] != SENT && v[1] != SENT && v[2] != SENT && v[3] != SENT;
        if (__all(ok)) break;
      }
      STAMP(12);
      float y[4];
#pragma unroll
      for (int i = 0; i < 4; ++i) { const u32 p = v[i]; float t = asf(p); t += dpp<ROR4>(t); t += dpp<ROR8>(t); y[i] = t; }
      const float r = sw16(sw32(y[0], y[2]), sw32(y[1], y[3]));
      if ((lane & 12) == 0) sh->red2[4 * (lane & 3) + (lane >> 4)][wave] = r;
      bar();
      if (STREAM && wave == 0) {
        const LAS float* rp = sh->red2[lane & 15];
        const f4 tt = *(const LAS f4*)rp + *(const LAS f4*)(rp + 4) + *(const LAS f4*)(rp + 8);
        const float f = rb((tt[0] + tt[1]) + (tt[2] + tt[3]));
        const float r0 = lo16(xr) + rb(hi16(gv) * f);
        if (lane < 16) c.out[16 * r2 + lane] = (u16)pk(r0, 0.f);
        STAMP(13);
      }
    }
  }
  STAMP(15);
  STAMP_OUT();
}

__global__ void __launch_bounds__(768, 3)
fused_kernel(const u16* __restrict__ x, const u16* __restrict__ kc, const u16* __restrict__ vc, const u16* __restrict__ wq,
             const u16* __restrict__ wo, const u16* __restrict__ wg, const u16* __restrict__ wu, const u16* __restrict__ wd,
             const u16* __restrict__ cv, const u16* __restrict__ wmod, u16* __restrict__ out, char* ws) {
  __shared__ Sh shm;
  LAS Sh* sh = (LAS Sh*)&shm;
  STAMP(0);
  const int lane = threadIdx.x & 63, wave = __builtin_amdgcn_readfirstlane(threadIdx.x >> 6);
  const int b = blockIdx.x, xc = b & 7, s = (b >> 3) & 7, hl = b >> 6, h = hl * 8 + xc;
  Ctx c; c.x = x; c.wg = wg; c.wu = wu; c.wd = wd; c.out = out; c.ws = ws; c.b = b; c.xc = xc; c.s = s; c.hl = hl; c.h = h; c.lane = lane; c.wave = wave;
  const rsrc_t rws = mkrs(ws, O_END);
  const rsrc_t rwo = mkrs(wo + (size_t)h * 128 * D, Z_WO ? 0 : 128 * D * 2);
  const int wo_vo = (16 * (lane >> 4) + (lane & 8)) * (D * 2) + (48 * wave + 6 * (lane & 7)) * 2;
#define LD_WO(i) __builtin_amdgcn_raw_buffer_load_b96(rwo, wo_vo, (64 * ((i) >> 3) + ((i) & 7)) * (D * 2), AUX_SH)
  const bool ffn = s < 6;
  const u32 gstr = (ffn && !Z_GU) ? FF * 2 : 0;
  const char* gup = (ffn && !Z_GU) ? (const char*)(((lane & 8) ? wu : wg) + (size_t)(64 * wave + 16 * (lane >> 4) + 2 * (lane & 4)) * FF + 576 * xc + 24 * (hl * 6 + s) + 6 * (lane & 3))
                        : (const char*)x;

  if (wave < 8) {
    const size_t slab = ((size_t)h * 8192 + s * 1024) * 128;
    const rsrc_t rk = mkrs(kc + slab, 262144), rv = mkrs(vc + slab, 262144);
    const int vo = (wave >> 2) * 131072 + (wave & 3) * 1024 + lane * 16;
#define LD_K(i) __builtin_amdgcn_raw_buffer_load_b128(rk, vo, (i) * 4096, NT)
#define LD_V(i) __builtin_amdgcn_raw_buffer_load_b128(rv, vo, (i) * 4096, NT)
    u4 kb[32], vb[32];
    bar();
#pragma unroll
    for (int i = 0; i < KQ; ++i) {
      if (i >= WK) VMCNT(WK - 1);
      if (i == WK) STAMP(1);
      kb[i] = LD_K(i);
      SB();
    }
    STAMP(2);
    lds_wait(&sh->flag[6]);
    STAMP(3);
    const u4 qv = *(const LAS u4*)(sh->q + 4 * (lane & 15));
    float sp[32], s0 = 0.f;
#define QK(i) do { float a_ = dot2(kb[i][0], qv[0], 0.f), b_ = dot2(kb[i][1], qv[1], 0.f); a_ = dot2(kb[i][2], qv[2], a_); b_ = dot2(kb[i][3], qv[3], b_); sp[i] = a_ + b_; PIN(sp[i]); } while (0)
    constexpr int NR = 32 - KQ;
    constexpr int PER = (32 - WK + NR - 1) / NR;
#pragma unroll
    for (int i = 0; i < NR; ++i) {
      VMCNT(WK - 1);
      kb[KQ + i] = LD_K(KQ + i);
      SB();
#pragma unroll
      for (int j = i * PER; j < (i + 1) * PER; ++j) {
        if (j < 32 - WK && j <= KQ + i - WK) { QK(j); }
      }
      SB();
    }
#pragma unroll
    for (int j = 0; j < 32 - WK; ++j) {
      const int st = j / PER;
      if (!(st < NR && j <= KQ + st - WK)) { QK(j); }
    }
    s0 = tred16(sp, lane);
    PIN(s0);
    SB();
#pragma unroll
    for (int i = 0; i < WK; ++i) {
      VMCNT(WK - 1);
      vb[i] = LD_V(i);
      SB();
      QK(32 - WK + i);
      SB();
    }
    vb[WK] = LD_V(WK);
    SB();
    STAMP(4);
    float s1 = tred16(sp + 16, lane);
    s0 = rb(rb(s0) * 0.08838834764831845f);
    s1 = rb(rb(s1) * 0.08838834764831845f);
    const float mw = wave_max(fmaxf(s0, s1));
    const float p0 = rb(__expf(s0 - mw)), p1 = rb(__expf(s1 - mw));
    const float lw = wave_sum(p0 + p1);
    if (lane == 0) { u2 w = {canon(asu(mw)), canon(asu(lw))}; *(volatile LAS u2*)&sh->ml[wave] = w; }
    SB();
    STAMP(5);
    float acc[8];
#pragma unroll
    for (int i = 0; i < 8; ++i) acc[i] = 0.f;
    u3 wr[16], gw[8];
    u4 WF[12];
    constexpr int WV = WK + 1;
#pragma unroll
    for (int i = 0; i < 32; ++i) {
      u32 pb;
      {
        const u32 src = asu(i < 16 ? p0 : p1);
        switch (i & 15) {
#define CASE(n) case n: pb = dppu<NBC + n>(src); break;
          CASE(0) CASE(1) CASE(2) CASE(3) CASE(4) CASE(5) CASE(6) CASE(7) CASE(8) CASE(9) CASE(10) CASE(11) CASE(12) CASE(13) CASE(14)
          default: pb = dppu<NBC + 15>(src); break;
#undef CASE
        }
      }
      const u32 pl = pb >> 16;
#pragma unroll
      for (int cc = 0; cc < 4; ++cc) { const u32 w = vb[i][cc]; acc[2 * cc] = dot2(w, pl, acc[2 * cc]); acc[2 * cc + 1] = dot2(w, pb, acc[2 * cc + 1]); }
#pragma unroll
      for (int cc = 0; cc < 8; ++cc) PIN(acc[cc]);
      SB();
      if (i + WV < 32) vb[i + WV] = LD_V(i + WV);
      if (i >= 2 && i < 18) wr[i - 2] = LD_WO(i - 2);
      if (i >= 27 && i < 31) {
#pragma unroll
        for (int j = 2 * (i - 27); j < 2 * (i - 27) + 2; ++j)
          gw[j] = NT_GU ? __builtin_nontemporal_load((const GAS u3a*)(gup + j * gstr)) : *(const GAS u3a*)(gup + j * gstr);
      }
      SB();
      if (i == 20) { frag<3>(wr, WF); for (int j = 0; j < 6; ++j) PIN(WF[j]); }
      if (i == 24) { frag<3>(wr + 8, WF + 6); for (int j = 6; j < 12; ++j) PIN(WF[j]); }
      if (i == 26) { for (u32 n = 0; n < SPIN_L; ++n) { if (((volatile LAS u32*)&sh->gml[0])[1] != SENT) break; __builtin_amdgcn_s_sleep(1); } FENCE(); }
      SB();
      if (i == 15) STAMP(6);
    }
    STAMP(7);
    const u2 g = *(volatile LAS u2*)&sh->gml[0];
    const u32 g0 = g[0], g1 = g[1];
    const float fw = __expf(mw - asf(g0)) * asf(g1);
    float t4[4];
#pragma unroll
    for (int i = 0; i < 4; ++i) t4[i] = sw32(acc[i], acc[i + 4]);
    f2 ow = {sw16(t4[0], t4[2]) * fw, sw16(t4[1], t4[3]) * fw};
    *(LAS f2*)&sh->op[wave][8 * (lane & 15) + 2 * (lane >> 4)] = ow;
    tail<true>(c, sh, rws, WF, gw, gup, gstr
    );
  } else {
    const u2 ep = __builtin_amdgcn_raw_buffer_load_b64(rws, O_EPOCH, 0, SC1);
    const bool job = h < 27;
    const int kind = h / 9, line = h - 9 * kind;
    if (wave < 11) {
      const int j = wave - 8, r0 = 192 * j + 6 * (lane >> 1);
      const rsrc_t rc = mkrs(cv, D * 2), rm = mkrs(wmod, (job && !Z_PRE) ? D * 6 * D * 2 : 0), rq = mkrs(wq, Z_PRE ? 0 : D * 4096 * 2);
      const u3 cr = __builtin_amdgcn_raw_buffer_load_b96(rc, r0 * 2, 0, 0);
      const int pa = kind == 0 ? 0 : kind == 1 ? 3 : 2, pb = kind == 0 ? 1 : kind == 1 ? 4 : 5;
      const int mo = r0 * (6 * D * 2) + (((lane & 1) ? pb : pa) * D + 64 * line + 8 * s) * 2;
      const int qo = r0 * 8192 + (128 * h + 16 * s + 8 * (lane & 1)) * 2;
      u4 wm[6], wqr[6];
#pragma unroll
      for (int i = 0; i < 6; ++i) wm[i] = __builtin_amdgcn_raw_buffer_load_b128(rm, mo, i * (6 * D * 2), AUX_SH);
#pragma unroll
      for (int i = 0; i < 6; ++i) wqr[i] = __builtin_amdgcn_raw_buffer_load_b128(rq, qo, i * 8192, AUX_SH);
      SB();
      bar();
      u32 act[3];
#pragma unroll
      for (int p = 0; p < 3; ++p) { const u32 w = cr[p]; act[p] = pk(silu(lo16(w)), silu(hi16(w))); }
      job_dot(wm, act, sh->mp[0][j], lane);
      asm volatile("s_waitcnt lgkmcnt(0)" ::: "memory");
      if (lane == 0) *(volatile LAS u32*)&sh->flag[j] = 1;
      STAMP(1);
      const u64 tag = (((u64)ep[1] << 32) | ep[0]) + 1;
      const u32 tl = (u32)tag << 16, th = (u32)(tag >> 16);
      const int ho = O_HG + xc * HG_STR + r0 * 8;
      for (u32 n = 0; n < SPIN_G; ++n) {
        FENCE();
        u4 g[3]; bool ok = true;
#pragma unroll
        for (int p = 0; p < 3; ++p) g[p] = __builtin_amdgcn_raw_buffer_load_b128(rws, ho + 16 * p, 0, SC1);
#pragma unroll
        for (int p = 0; p < 3; ++p) {
          ok = ok && (g[p][0] & 0xffff0000u) == tl && g[p][1] == th && (g[p][2] & 0xffff0000u) == tl && g[p][3] == th;
          const u32 a = g[p][0], bb = g[p][2];
          act[p] = plo(a, bb);
        }
        if (__all(ok)) break;
        __builtin_amdgcn_s_sleep(2);
      }
      STAMP(2);
      job_dot(wqr, act, sh->mp[1][j], lane);
      asm volatile("s_waitcnt lgkmcnt(0)" ::: "memory");
      if (lane == 0) *(volatile LAS u32*)&sh->flag[3 + j] = 1;
      STAMP(3);
      for (u32 n = 0; n < SPIN_L; ++n) { if (((volatile LAS u32*)&sh->gml[0])[1] != SENT) break; __builtin_amdgcn_s_sleep(8); }
      FENCE();
    } else {
      const rsrc_t rx = mkrs(x, D * 2);
      u32 xs[9];
#pragma unroll
      for (int i = 0; i < 9; ++i) xs[i] = __builtin_amdgcn_raw_buffer_load_b16(rx, (64 * i + lane) * 2, 0, 0);
      const u32 xo = __builtin_amdgcn_raw_buffer_load_b16(rx, (64 * line + 8 * s + (lane & 7)) * 2, 0, 0);
      const u4 s4 = {SENT, SENT, SENT, SENT};
      const int rho = xc + 8 * (hl * 2 + (s - 6));
#pragma unroll
      for (int i = 0; i < 3; ++i) {
        const int ch = 64 * i + lane;
        __builtin_amdgcn_raw_buffer_store_b128(s4, rws, O_YA + (ch >> 2) * 16384 + b * 64 + (ch & 3) * 16, 0, SC1);
        if (ffn && ch < 144) __builtin_amdgcn_raw_buffer_store_b128(s4, rws, O_FC + (ch >> 2) * 12288 + (xc * 24 + hl * 6 + s) * 64 + (ch & 3) * 16, 0, SC1);
      }
      if (lane < 4) __builtin_amdgcn_raw_buffer_store_b128(s4, rws, O_ML + h * 512 + s * 64 + lane * 16, 0, SC1);
      if (!ffn && rho < 48 && lane < 16) { const u3 s3 = {SENT, SENT, SENT}; __builtin_amdgcn_raw_buffer_store_b96(s3, rws, O_X2 + (lane >> 1) * X2_STR + rho * 24 + (lane & 1) * 12, 0, SC1); }
      if (lane < 8) sh->flag[lane] = 0;
      if (lane < 20) ((LAS u32*)sh->ml)[lane] = SENT;
      if (lane < 48) { f2 z = {0.f, 0.f}; *(LAS f2*)&sh->gp[lane][18] = z; }
      SB();
      bar();
      const u64 tag = (((u64)ep[1] << 32) | ep[0]) + 1;
      const u32 tl = (u32)tag << 16, th = (u32)(tag >> 16);
      if (job) {
        float ln = 0.f;
        if (kind == 0) {
          float sum = 0.f;
#pragma unroll
          for (int i = 0; i < 9; ++i) sum += lo16(xs[i]);
          const float mean = wave_sum(sum) * (1.f / D);
          float sq = 0.f;
#pragma unroll
          for (int i = 0; i < 9; ++i) { const float d = lo16(xs[i]) - mean; sq = fmaf(d, d, sq); }
          const float var = wave_sum(sq) * (1.f / D);
          const float den = rb(__builtin_sqrtf(rb(rb(var) + 1e-5f)));
          ln = rb(rb(lo16(xo) - rb(mean)) / den);
        }
        lds_wait(&sh->flag[0]); lds_wait(&sh->flag[1]); lds_wait(&sh->flag[2]);
        const float ma = rb(sh->mp[0][0][lane & 15] + sh->mp[0][1][lane & 15] + sh->mp[0][2][lane & 15]);
        const float mb = dpp<SHL + 8>(ma);
        const int col = 64 * line + 8 * s + (lane & 7);
        if (kind == 0) {
          const float hv = rb(rb(ln * rb(1.f + mb)) + ma);
          const u32 h0 = asu(hv) >> 16, h1 = dppu<SHL + 1>(h0);
          const u4 g = {h0 | tl, th, h1 | tl, th};
          if ((lane & 9) == 0) {
            __builtin_amdgcn_raw_buffer_store_b128(g, rws, O_HG + (lane >> 4) * HG_STR + col * 8, 0, SC1);
            __builtin_amdgcn_raw_buffer_store_b128(g, rws, O_HG + (4 + (lane >> 4)) * HG_STR + col * 8, 0, SC1);
          }
        } else {
          const u32 w = pk(ma, kind == 1 ? 1.f + mb : mb);
          if (lane < 8) __builtin_amdgcn_raw_buffer_store_b32(w, rws, O_MODL + (kind - 1) * 2560 + col * 4, 0, SC1);
        }
      }
      STAMP(1);
      VMCNT(0);
      if (lane == 0) { const u2 t = {(u32)tag, (u32)(tag >> 32)}; __builtin_amdgcn_raw_buffer_store_b64(t, rws, O_RDY + b * 8, 0, SC1); }
      lds_wait(&sh->flag[3]); lds_wait(&sh->flag[4]); lds_wait(&sh->flag[5]);
      {
        const float qv = rb(sh->mp[1][0][lane & 15] + sh->mp[1][1][lane & 15] + sh->mp[1][2][lane & 15]);
        const u32 h0 = asu(qv) >> 16, h1 = dppu<SHL + 1>(h0);
        const u4 g = {h0 | tl, th, h1 | tl, th};
        if (lane < 16 && (lane & 1) == 0) __builtin_amdgcn_raw_buffer_store_b128(g, rws, O_QG + h * 1024 + (16 * s + lane) * 8, 0, SC1);
      }
      STAMP(2);
      {
        u4 g;
        for (u32 n = 0; n < SPIN_G; ++n) {
          FENCE();
          g = __builtin_amdgcn_raw_buffer_load_b128(rws, O_QG + h * 1024 + lane * 16, 0, SC1);
          const bool ok = (g[0] & 0xffff0000u) == tl && g[1] == th && (g[2] & 0xffff0000u) == tl && g[3] == th;
          if (__all(ok)) break;
        }
        const u32 a = g[0], bb = g[2];
        sh->q[lane] = plo(a, bb);
        asm volatile("s_waitcnt lgkmcnt(0)" ::: "memory");
        if (lane == 0) *(volatile LAS u32*)&sh->flag[6] = 1;
      }
      STAMP(3);
      for (u32 n = 0; n < SPIN_G; ++n) {
        FENCE();
        const u4 g0 = __builtin_amdgcn_raw_buffer_load_b128(rws, O_RDY + lane * 16, 0, SC1);
        const u4 g1 = __builtin_amdgcn_raw_buffer_load_b128(rws, O_RDY + 1024 + lane * 16, 0, SC1);
        const u32 t0 = (u32)tag, t1 = (u32)(tag >> 32);
        const bool ok = g0[0] == t0 && g0[1] == t1 && g0[2] == t0 && g0[3] == t1 && g1[0] == t0 && g1[1] == t1 && g1[2] == t0 && g1[3] == t1;
        if (__all(ok)) break;
        __builtin_amdgcn_s_sleep(4);
      }
      FENCE();
      if (b == 0 && lane == 0) { const u2 t = {(u32)tag, (u32)(tag >> 32)}; __builtin_amdgcn_raw_buffer_store_b64(t, rws, O_EPOCH, 0, SC1); }
      {
        const int mo = O_MODL + (ffn ? 0 : 2560) + lane * 16;
        const u4 m0 = __builtin_amdgcn_raw_buffer_load_b128(rws, mo, 0, SC1);
        const u4 m1 = __builtin_amdgcn_raw_buffer_load_b128(rws, mo + 1024, 0, SC1);
        *(LAS u4*)&sh->sc[lane * 4] = m0;
        *(LAS u4*)&sh->sc[256 + lane * 4] = m1;
        if (lane < 16) *(LAS u4*)&sh->sc[512 + lane * 4] = __builtin_amdgcn_raw_buffer_load_b128(rws, mo + 2048, 0, SC1);
      }
      STAMP(4);
      {
        u4 m;
        for (u32 n = 0; n < SPIN_L; ++n) {
          m = *(volatile LAS u4*)&sh->ml[2 * (lane & 3)];
          if (__all(m[0] != SENT && m[1] != SENT && m[2] != SENT && m[3] != SENT)) break;
          __builtin_amdgcn_s_sleep(2);
        }
        if (lane < 4) __builtin_amdgcn_raw_buffer_store_b128(m, rws, O_ML + h * 512 + s * 64 + lane * 16, 0, SC1);
        STAMP(5);
        u2 g;
        for (u32 n = 0; n < SPIN_G; ++n) {
          FENCE();
          g = __builtin_amdgcn_raw_buffer_load_b64(rws, O_ML + h * 512 + lane * 8, 0, SC1);
          if (__all(g[0] != SENT && g[1] != SENT)) break;
          __builtin_amdgcn_s_sleep(2);
        }
        const u32 g0 = g[0], g1 = g[1];
        const float M = wave_max(asf(g0));
        const float L = wave_sum(asf(g1) * __expf(asf(g0) - M));
        if (lane == 0) { const u2 w = {asu(M), canon(asu(1.f / L))}; *(volatile LAS u2*)&sh->gml[0] = w; }
      }
      STAMP(6);
    }
    u3 wr[16], gw[8];
    u4 WF[12];
#pragma unroll
    for (int i = 0; i < 16; ++i) {
      if (i >= 2) VMCNT(1);
      wr[i] = LD_WO(i);
      SB();
    }
    frag<3>(wr, WF);
    frag<3>(wr + 8, WF + 6);
#pragma unroll
    for (int j = 0; j < 12; ++j) PIN(WF[j]);
    SB();
    STAMP_V(7, WF[0]);
    tail<false>(c, sh, rws, WF, gw, gup, gstr
    );
  }
}

extern "C" void kernel_launch(void* const* d_in, const int* in_sizes, int n_in, void* d_out, int out_size, void* d_ws, size_t ws_size,
                              hipStream_t stream) {
  fused_kernel<<<256, 768, 0, stream>>>((const u16*)d_in[0], (const u16*)d_in[1], (const u16*)d_in[2], (const u16*)d_in[3],
                                        (const u16*)d_in[4], (const u16*)d_in[6], (const u16*)d_in[7], (const u16*)d_in[8],
                                        (const u16*)d_in[9], (const u16*)d_in[10], (u16*)d_out, (char*)d_ws);
}
```

```cpp
#include <hip/hip_runtime.h>
#include <stdint.h>

#define STAMPS 0
#define WK 3
#define WK2 3
#define T2 48
#define PV0 4
#define WO0 41
#define GN 0
#define RX 16
#define YSTR 128

typedef unsigned u32;
typedef unsigned long long u64;
typedef unsigned short u16;
typedef u32 u1 __attribute__((ext_vector_type(1)));
typedef u32 u2 __attribute__((ext_vector_type(2)));
typedef u32 u3 __attribute__((ext_vector_type(3)));
typedef u32 u4 __attribute__((ext_vector_type(4)));
typedef float f2 __attribute__((ext_vector_type(2)));
typedef float f4 __attribute__((ext_vector_type(4)));
typedef __bf16 bf2 __attribute__((ext_vector_type(2)));
typedef __bf16 bf8 __attribute__((ext_vector_type(8)));
typedef __amdgpu_buffer_rsrc_t rsrc_t;
#define LAS __attribute__((address_space(3)))
#define GAS __attribute__((address_space(1)))
#define DI __device__ __forceinline__

constexpr int D = 576, FF = 4608;
constexpr int SC1 = 16, NT = 2;
constexpr u32 SPIN_G = 1u << 17, SPIN_L = 1u << 20;

constexpr int O_EPOCH = 0;
constexpr int O_SC = 4096;
constexpr int O_ML = 12288;
constexpr int O_MOD = 16384, MOD_STR = 3456 * 8;
constexpr int O_X2 = 131072;
constexpr int O_QP = 262144;
constexpr int O_YA = O_QP + 262144;
constexpr int O_FC = O_YA + 36 * 256 * YSTR;
constexpr int O_END = O_FC + 36 * 144 * YSTR;
constexpr int O_STAMP = 4 << 20;

DI float asf(u32 x) { return __uint_as_float(x); }
DI u32 asu(float x) { return __float_as_uint(x); }
DI u32 pk(float lo, float hi) { f2 t = {lo, hi}; bf2 r = __builtin_convertvector(t, bf2); return __builtin_bit_cast(u32, r); }
DI float rb(float x) { return asf(pk(0.f, x)); }
DI float lo16(u32 x) { return asf(x << 16); }
DI float hi16(u32 x) { return asf(x & 0xffff0000u); }
template <int C> DI u32 dppu(u32 x) { return (u32)__builtin_amdgcn_update_dpp(0, (int)x, C, 0xf, 0xf, true); }
template <int C> DI float dpp(float x) { return asf(dppu<C>(asu(x))); }
constexpr int ROR4 = 0x124, ROR8 = 0x128, HMIR = 0x141, QX1 = 0xB1, QX2 = 0x4E, SHL = 0x100, NBC = 0x150;
DI u32 nbc(u32 src, int n) {
  switch (n) {
#define CASE(n) case n: return dppu<NBC + n>(src);
    CASE(0) CASE(1) CASE(2) CASE(3) CASE(4) CASE(5) CASE(6) CASE(7) CASE(8) CASE(9) CASE(10) CASE(11) CASE(12) CASE(13) CASE(14)
#undef CASE
    default: return dppu<NBC + 15>(src);
  }
}
DI float dot2(u32 a, u32 b, float c) { return __builtin_amdgcn_fdot2_f32_bf16(__builtin_bit_cast(bf2, a), __builtin_bit_cast(bf2, b), c, false); }
DI float sw32(float a, float b) { auto r = __builtin_amdgcn_permlane32_swap(asu(a), asu(b), false, false); u32 r0 = r[0], r1 = r[1]; return asf(r0) + asf(r1); }
DI float sw16(float a, float b) { auto r = __builtin_amdgcn_permlane16_swap(asu(a), asu(b), false, false); u32 r0 = r[0], r1 = r[1]; return asf(r0) + asf(r1); }
DI float mx32(float a) { auto r = __builtin_amdgcn_permlane32_swap(asu(a), asu(a), false, false); u32 r0 = r[0], r1 = r[1]; return fmaxf(asf(r0), asf(r1)); }
DI float mx16(float a) { auto r = __builtin_amdgcn_permlane16_swap(asu(a), asu(a), false, false); u32 r0 = r[0], r1 = r[1]; return fmaxf(asf(r0), asf(r1)); }
DI float row_sum(float v) { v += dpp<QX1>(v); v += dpp<QX2>(v); v += dpp<ROR4>(v); return v + dpp<ROR8>(v); }
DI float wave_sum(float v) { v = row_sum(v); v = sw16(v, v); return sw32(v, v); }
DI float wave_max(float v) {
  v = fmaxf(v, dpp<QX1>(v)); v = fmaxf(v, dpp<QX2>(v)); v = fmaxf(v, dpp<ROR4>(v)); v = fmaxf(v, dpp<ROR8>(v));
  return mx32(mx16(v));
}
DI float sum8(float v) { v += dpp<QX1>(v); v += dpp<QX2>(v); return v + dpp<HMIR>(v); }
DI float max8(float v) { v = fmaxf(v, dpp<QX1>(v)); v = fmaxf(v, dpp<QX2>(v)); return fmaxf(v, dpp<HMIR>(v)); }
DI u32 plo(u32 a, u32 b) { return __builtin_amdgcn_perm(b, a, 0x05040100u); }
DI u32 phi(u32 a, u32 b) { return __builtin_amdgcn_perm(b, a, 0x07060302u); }
DI f4 mfma(u4 a, u4 b, f4 c) { return __builtin_amdgcn_mfma_f32_16x16x32_bf16(__builtin_bit_cast(bf8, a), __builtin_bit_cast(bf8, b), c, 0, 0, 0); }
DI rsrc_t mkrs(const void* p, u32 n) { return __builtin_amdgcn_make_buffer_rsrc((void*)p, 0, n, 0x00020000); }
#define FENCE() asm volatile("" ::: "memory")
#define SB() __builtin_amdgcn_sched_barrier(0)
#define PIN(v) asm volatile("" : "+v"(v))
#define VMCNT(n) asm volatile("s_waitcnt vmcnt(%0)" ::"n"(n) : "memory")
#define LDSW() asm volatile("s_waitcnt lgkmcnt(0)" ::: "memory")
DI void bar() { asm volatile("s_waitcnt lgkmcnt(0)\n\ts_barrier" ::: "memory"); }
DI float silu(float v) { return v * __builtin_amdgcn_rcpf(1.f + __expf(-v)); }
DI u4 ld128(rsrc_t r, int o) { return __builtin_amdgcn_raw_buffer_load_b128(r, o, 0, SC1); }
DI u2 ld64(rsrc_t r, int o) { return __builtin_amdgcn_raw_buffer_load_b64(r, o, 0, SC1); }
DI void st128(u4 v, rsrc_t r, int o) { __builtin_amdgcn_raw_buffer_store_b128(v, r, o, 0, SC1); }
DI float ln_rstd(float var) { return __builtin_amdgcn_rcpf(rb(__builtin_amdgcn_sqrtf(rb(rb(var) + 1e-5f)))); }

struct __attribute__((aligned(16))) Sh {
  float gp[64][20];
  float red[16][12];
  float red2[16][12];
  float mp[3][64];
  float wt[256];
  u32 sc[576];
  u32 ob[8][72];
  u32 q[64];
  f2 st[16];
  f2 ml[8];
  f2 gml;
  u32 tag;
  u32 pad;
  u32 flag[16];
};

#define STAMP(i)
#define STAMP_OUT()

template <int S> DI void lds_wait(volatile LAS u32* f) {
#pragma nounroll
  for (u32 n = 0; n < SPIN_L; ++n) { if (*f != 0) break; __builtin_amdgcn_s_sleep(S); }
  FENCE();
}
DI void lds_set(LAS u32* f, int lane) { LDSW(); if (lane == 0) *(volatile LAS u32*)f = 1; }

DI float tred16(const float* s, int lane) {
  float t[8], u[4], v[2];
  const bool b3 = lane & 8, b2 = lane & 4, b1 = lane & 2, b0 = lane & 1;
#pragma unroll
  for (int i = 0; i < 8; ++i) t[i] = (b3 ? s[i + 8] : s[i]) + dpp<ROR8>(b3 ? s[i] : s[i + 8]);
#pragma unroll
  for (int i = 0; i < 4; ++i) u[i] = (b2 ? t[i + 4] : t[i]) + dpp<HMIR>(b2 ? t[i] : t[i + 4]);
#pragma unroll
  for (int i = 0; i < 2; ++i) v[i] = (b1 ? u[i + 2] : u[i]) + dpp<QX2>(b1 ? u[i] : u[i + 2]);
  return (b0 ? v[1] : v[0]) + dpp<QX1>(b0 ? v[0] : v[1]);
}

template <int C, typename V> DI void frag(const V* L, u4* F) {
#pragma unroll
  for (int c = 0; c < C; ++c) {
    u4 a, b;
#pragma unroll
    for (int j = 0; j < 4; ++j) { const u32 x = L[2 * j][c], y = L[2 * j + 1][c]; a[j] = plo(x, y); b[j] = phi(x, y); }
    PIN(a); PIN(b);
    F[2 * c] = a; F[2 * c + 1] = b;
  }
}

DI u4 gather8(float v) {
  const u32 d = pk(v, dpp<SHL + 1>(v));
  u4 a; a[0] = d; a[1] = dppu<SHL + 2>(d); a[2] = dppu<SHL + 4>(d); a[3] = dppu<SHL + 6>(d);
  return a;
}

template <int I> struct IC { static constexpr int value = I; };
template <int I, int N, class F> DI void sfor(F&& f) { if constexpr (I < N) { f(IC<I>{}); sfor<I + 1, N>(f); } }

struct Ctx {
  const u16* wd; u16* out; char* ws; const char* gup;
  int b, pi, lane, wave;
};
DI void swap16(u32 d, u32& r0, u32& r1) { auto r = __builtin_amdgcn_permlane16_swap(d, d, false, false); r0 = r[0]; r1 = r[1]; }
DI void swap32(u32 d, u32& r0, u32& r1) { auto r = __builtin_amdgcn_permlane32_swap(d, d, false, false); r0 = r[0]; r1 = r[1]; }
#define LD_GU(p, j) __builtin_nontemporal_load((const GAS u4*)((p) + (j) * (FF * 2)))

template <bool STREAM>
DI void tail(const Ctx& c, LAS Sh* sh, rsrc_t rws, const u4* WF, u4* gw
) {
  const int lane = c.lane, wave = c.wave;
  char* ws = c.ws; (void)ws;
  const u32 tag = sh->tag;
  int yo = O_YA + c.pi * YSTR + (STREAM ? (4 * wave + ((lane & 7) >> 1)) * 256 * YSTR + (2 * (lane & 1) + (lane >> 5)) * 16
                                        : (24 + wave) * 256 * YSTR + (lane >> 1) * 16);
  PIN(yo);
  bar();
  STAMP(8);
  {
    constexpr int NTL = STREAM ? 8 : 2;
    const LAS u32* ob = &sh->ob[(lane & 15) >> 1][8 * (lane >> 4) + 4 * (lane & 1)];
    const u4 a0 = *(const LAS u4*)ob, a1 = *(const LAS u4*)(ob + 32);
    f4 dd[NTL];
#pragma unroll
    for (int j = 0; j < NTL; ++j) { f4 z = {0.f, 0.f, 0.f, 0.f}; dd[j] = mfma(a0, WF[j], z); }
#pragma unroll
    for (int j = 0; j < NTL; ++j) dd[j] = mfma(a1, WF[NTL + j], dd[j]);
    const bool half = lane & 8;
    float v[NTL];
#pragma unroll
    for (int j = 0; j < NTL; ++j) v[j] = half ? dd[j][1] + dd[j][3] : dd[j][0] + dd[j][2];
    u32 r0, r1;
    if constexpr (STREAM) {
      float t[4], u[2];
#pragma unroll
      for (int i = 0; i < 4; ++i) t[i] = sw32(v[i], v[i + 4]);
#pragma unroll
      for (int i = 0; i < 2; ++i) { u[i] = sw16(t[i], t[i + 2]); u[i] += dpp<ROR8>(u[i]); }
      swap16(pk(u[0], u[1]), r0, r1);
      const u4 o = {r0, tag, r1, tag};
      if ((lane & 24) == 0) st128(o, rws, yo);
    } else {
      float t = sw32(v[0], v[1]);
      t = sw16(t, t); t += dpp<ROR8>(t);
      swap32(asu(t), r0, r1);
      const u32 d = pk(asf(r0), asf(r1));
      const u4 o = {d, tag, dppu<SHL + 1>(d), tag};
      if ((lane & 57) == 0) st128(o, rws, yo);
    }
  }
  STAMP(9);
  if (c.b >= 112) {
    const int f = c.b - 112;
    bar();
    if (STREAM || wave == 8) {
#pragma unroll
      for (int j = STREAM ? GN : 0; j < 8; ++j) gw[j] = LD_GU(c.gup, j);
      SB();
      int o1 = O_X2 + (f % RX) * 4608 + (4 * wave + (lane >> 4)) * 128 + ((lane & 15) >> 1) * 8;
      const float sbv = lo16(((const LAS u16*)sh->sc)[64 * wave + lane]);
      float cbv = rb(1.f + lo16(((const LAS u16*)sh->sc)[D + 64 * wave + lane]));
      PIN(cbv);
      u4 GF[8];
      frag<4>(gw, GF);
      SB();
      STAMP(10);
      u2 xr;
#pragma nounroll
      for (u32 n = 0; n < SPIN_G; ++n) {
        FENCE(); PIN(o1);
        xr = ld64(rws, o1);
        if (__all(xr[1] == tag)) break;
      }
      STAMP(11);
      const u32 x0 = xr[0];
      const float xv = (lane & 1) ? hi16(x0) : lo16(x0);
      { const f2 w = {wave_sum(xv), wave_sum(xv * xv)}; sh->st[wave] = w; }
      bar();
      const f2 st = sh->st[lane & 15];
      const float mean = row_sum(st[0]) * (1.f / D), e2 = row_sum(st[1]) * (1.f / D);
      const float rs = __builtin_amdgcn_rsqf(fmaxf(fmaf(-mean, mean, e2), 0.f) + 1e-5f);
      const u4 a = gather8(fmaf(xv - mean, rs * cbv, sbv));
      f4 dd[8];
#pragma unroll
      for (int j = 0; j < 8; ++j) { f4 z = {0.f, 0.f, 0.f, 0.f}; dd[j] = mfma(a, GF[j], z); }
      const int par = (lane >> 2) & 1;
      if ((lane >> 4) == 2 * par) {
        LAS float* gp = &sh->gp[32 * ((lane >> 3) & 1) + 8 * (lane & 3)][2 * wave + par];
#pragma unroll
        for (int j = 0; j < 8; ++j) gp[20 * j] = dd[j][0];
      }
      bar();
      STAMP(12);
    } else if (!STREAM) {
      const int dw = wave - 9, g = lane >> 4;
      const rsrc_t rwd = mkrs(c.wd + (size_t)(576 * (c.b & 7) + 32 * (f >> 3)) * D, 32 * D * 2);
      u2 wd[3][8];
      const int vo = 8 * g * (D * 2) + (192 * dw + 4 * (lane & 15)) * 2;
      __builtin_amdgcn_s_sleep(4);
#pragma unroll
      for (int j = 0; j < 8; ++j)
#pragma unroll
        for (int e = 0; e < 3; ++e) wd[e][j] = __builtin_amdgcn_raw_buffer_load_b64(rwd, vo + e * 128, j * (D * 2), NT);
      SB();
      int fo = O_FC + ((12 * dw + ((lane & 15) >> 2)) * 144 + f) * YSTR + (lane & 3) * 16;
      PIN(fo);
      u4 DF[12];
#pragma unroll
      for (int e = 0; e < 3; ++e) frag<2>(wd[e], DF + 4 * e);
      SB();
      STAMP(10);
      bar();
      bar();
      STAMP(12);
      const LAS float* gp = sh->gp[32 * ((lane >> 3) & 1) + 8 * g + (lane & 7)];
      f4 t[5];
#pragma unroll
      for (int i = 0; i < 5; ++i) t[i] = *(const LAS f4*)(gp + 4 * i);
      const f4 tt = (t[0] + t[1]) + (t[2] + t[3]) + t[4];
      const float gs = (tt[0] + tt[1]) + (tt[2] + tt[3]);
      const float us = dpp<SHL + 8>(gs);
      const u4 a = gather8(silu(gs) * us);
#pragma unroll
      for (int e = 0; e < 3; ++e) {
        float r[4];
#pragma unroll
        for (int j = 0; j < 4; ++j) { f4 z = {0.f, 0.f, 0.f, 0.f}; const f4 t4 = mfma(a, DF[4 * e + j], z); r[j] = t4[0]; }
        const u4 o = {pk(r[0], r[1]), tag, pk(r[2], r[3]), tag};
        if (lane < 16) st128(o, rws, fo + e * 4 * 144 * YSTR);
      }
      STAMP(13);
    }
  } else if (c.b >= 64 && c.b < 100) {
    const int rho = c.b - 64;
    const int l16 = lane & 15;
    float xin = 0.f, ga = 0.f, gb = 0.f;
    if (STREAM) {
      if (wave < RX / 4) { ga = lo16(((const LAS u16*)sh->sc)[l16]); xin = lo16(((const LAS u16*)sh->sc)[16 + l16]); gb = lo16(((const LAS u16*)sh->sc)[48 + l16]); }
      const float w0 = sh->wt[32 * wave + (lane >> 2)], w1 = sh->wt[32 * wave + 16 + (lane >> 2)];
      int o = O_YA + (rho * 256 + 32 * wave + (lane >> 2)) * YSTR + (lane & 3) * 16;
      u4 v0 = {0u, ~tag, 0u, ~tag}, v1 = v0;
#pragma nounroll
      for (u32 n = 0; n < SPIN_G; ++n) {
        const int oa = (v0[1] == tag) & (v0[3] == tag) ? O_END : o, ob = (v1[1] == tag) & (v1[3] == tag) ? O_END : o + 16 * YSTR;
        if (__all((oa == O_END) & (ob == O_END))) break;
        FENCE();
        const u4 n0 = ld128(rws, oa), n1 = ld128(rws, ob);
        v0 = oa == O_END ? v0 : n0; v1 = ob == O_END ? v1 : n1;
      }
      STAMP(10);
      const u32 a0 = v0[0], a2 = v0[2], b0 = v1[0], b2 = v1[2];
      float y[4] = {fmaf(lo16(a0), w0, lo16(b0) * w1), fmaf(hi16(a0), w0, hi16(b0) * w1), fmaf(lo16(a2), w0, lo16(b2) * w1), fmaf(hi16(a2), w0, hi16(b2) * w1)};
      float r = sw16(sw32(y[0], y[2]), sw32(y[1], y[3]));
      r += dpp<ROR4>(r); r += dpp<ROR8>(r);
      if ((lane & 12) == 0) sh->red[4 * (lane & 3) + (lane >> 4)][wave] = r;
    }
    bar();
    float x2 = 0.f;
    if (STREAM && wave < RX / 4) {
      const LAS float* rp = sh->red[l16];
      const f4 tt = *(const LAS f4*)rp + *(const LAS f4*)(rp + 4);
      x2 = rb(fmaf(ga, (tt[0] + tt[1]) + (tt[2] + tt[3]), xin));
      const u32 d = phi(asu(x2), dppu<SHL + 1>(asu(x2)));
      const u4 o = {d, tag, dppu<SHL + 2>(d), tag};
      if ((lane & 3) == 0) st128(o, rws, O_X2 + (4 * wave + (lane >> 4)) * 4608 + rho * 128 + l16 * 4);
      STAMP(11);
    }
    if (STREAM && wave == 7 && rho == 0 && lane == 0) __builtin_amdgcn_raw_buffer_store_b32(tag, rws, O_EPOCH, 0, SC1);
    if (STREAM || wave == 8) {
      int o = O_FC + (rho * 144 + 16 * wave + (lane >> 2)) * YSTR + (lane & 3) * 16;
      u4 v;
#pragma nounroll
      for (u32 n = 0; n < SPIN_G; ++n) {
        FENCE(); PIN(o);
        v = ld128(rws, o);
        if (__all((v[1] == tag) & (v[3] == tag))) break;
      }
      STAMP(12);
      const u32 a0 = v[0], a2 = v[2];
      float r = sw16(sw32(lo16(a0), lo16(a2)), sw32(hi16(a0), hi16(a2)));
      r += dpp<ROR4>(r); r += dpp<ROR8>(r);
      if ((lane & 12) == 0) sh->red2[4 * (lane & 3) + (lane >> 4)][wave] = r;
    }
    bar();
    if (STREAM && wave == 0) {
      const LAS float* rp = sh->red2[l16];
      const f4 tt = *(const LAS f4*)rp + *(const LAS f4*)(rp + 4);
      const float r0 = fmaf(gb, ((tt[0] + tt[1]) + (tt[2] + tt[3])) + rp[8], x2);
      if (lane < 16) c.out[16 * rho + lane] = (u16)pk(r0, 0.f);
      STAMP(13);
    }
  }
  STAMP(15);
  STAMP_OUT();
}

constexpr int win(int t) { return t < T2 ? WK : (WK + t - T2 + 1 < WK2 ? WK + t - T2 + 1 : WK2); }
constexpr int extras_after(int t) { return ((t >= WO0 && t < WO0 + 16) ? 1 : 0) + (t >= 64 - GN ? 1 : 0); }
constexpr int issued_before(int t) { int n = 0; for (int i = 0; i < t; ++i) n += 1 + extras_after(i); return n; }
constexpr int thr(int t) { return issued_before(t) - issued_before(t - win(t)) - 1; }
constexpr int QS = 32 + PV0 - 1;
constexpr int pv_done(int t) {
  int n = 0;
  for (int i = QS + 5; i <= t; ++i) { const int av = i - 32 - win(i) + 1; n = n + 3 < av ? n + 3 : (av > n ? av : n); }
  return n;
}
constexpr int landed(int t) { int i = t + 1; while (i < 64 && i - win(i) <= t) ++i; return i; }

__global__ void __launch_bounds__(768, 3)
fused_kernel(const u16* __restrict__ x, const u16* __restrict__ kc, const u16* __restrict__ vc, const u16* __restrict__ wq,
             const u16* __restrict__ wo, const u16* __restrict__ wg, const u16* __restrict__ wu, const u16* __restrict__ wd,
             const u16* __restrict__ cv, const u16* __restrict__ wmod, u16* __restrict__ out, char* ws) {
  __shared__ Sh shm;
  LAS Sh* sh = (LAS Sh*)&shm;
  STAMP(0);
  const int lane = threadIdx.x & 63, wave = __builtin_amdgcn_readfirstlane(threadIdx.x >> 6);
  const int b = blockIdx.x, xc = b & 7, ci = b >> 3, s = ci & 7, h = (ci >> 3) * 8 + xc;
  const bool ffn = b >= 112;
  Ctx c; c.wd = wd; c.out = out; c.ws = ws; c.b = b; c.pi = 8 * h + s; c.lane = lane; c.wave = wave;
  const rsrc_t rws = mkrs(ws, O_END);
  const rsrc_t rwo = mkrs(wo + (size_t)h * 128 * D, 128 * D * 2);
  const int wo_row = (16 * (lane >> 4) + (lane & 8)) * (D * 2);
  c.gup = (const char*)(((lane & 8) ? wu : wg) + (size_t)(64 * (wave < 8 ? wave : 8) + 16 * (lane >> 4) + 2 * (lane & 4)) * FF + 576 * xc + 32 * (ffn ? ci - 14 : 0) + 8 * (lane & 3));

  if (wave < 8) {
    const size_t slab = ((size_t)h * 8192 + s * 1024) * 128;
    const rsrc_t rk = mkrs(kc + slab, 262144), rv = mkrs(vc + slab, 262144);
    const int vo = (wave >> 2) * 131072 + (wave & 3) * 1024 + lane * 16;
    const int wo_vo = wo_row + 128 * wave + 16 * (lane & 7);
    u4 kb[32], vb[32], qv, gw[8], WF[16], wr[16];
    float sp[32], s0 = 0.f, mw = 0.f, p0 = 0.f, p1 = 0.f, acc[8];
#pragma unroll
    for (int i = 0; i < 8; ++i) acc[i] = 0.f;
#define QK(i) do { float a_ = dot2(kb[i][0], qv[0], 0.f), b_ = dot2(kb[i][1], qv[1], 0.f); a_ = dot2(kb[i][2], qv[2], a_); b_ = dot2(kb[i][3], qv[3], b_); sp[i] = a_ + b_; PIN(sp[i]); } while (0)
#define PV(i) do { const u32 pb_ = nbc(asu((i) < 16 ? p0 : p1), (i) & 15), pl_ = pb_ >> 16; \
      _Pragma("unroll") for (int cc = 0; cc < 4; ++cc) { const u32 w_ = vb[i][cc]; acc[2 * cc] = dot2(w_, pl_, acc[2 * cc]); acc[2 * cc + 1] = dot2(w_, pb_, acc[2 * cc + 1]); } \
      _Pragma("unroll") for (int cc = 0; cc < 8; ++cc) PIN(acc[cc]); } while (0)
    sfor<0, 64>([&](auto T) __attribute__((always_inline)) {
      constexpr int t = T.value;
      if constexpr (t >= WK) VMCNT(thr(t));
      if constexpr (t == WK) STAMP(1);
      if constexpr (t < 32) kb[t] = __builtin_amdgcn_raw_buffer_load_b128(rk, vo, t * 4096, NT);
      else vb[t - 32] = __builtin_amdgcn_raw_buffer_load_b128(rv, vo, (t - 32) * 4096, NT);
      if constexpr (t >= WO0 && t < WO0 + 16) wr[t - WO0] = __builtin_amdgcn_raw_buffer_load_b128(rwo, wo_vo, (64 * ((t - WO0) >> 3) + ((t - WO0) & 7)) * (D * 2), NT);
      if constexpr (t >= 64 - GN) gw[t - 64 + GN] = LD_GU(c.gup, t - 64 + GN);
      SB();
      if constexpr (t == WK - 1) bar();
      if constexpr (t == QS) {
        STAMP(2);
        lds_wait<1>(&sh->flag[3]);
        STAMP(3);
        qv = *(const LAS u4*)(sh->q + 4 * (lane & 15));
      }
      if constexpr (t >= QS && t < QS + 4) {
#pragma unroll
        for (int j = 8 * (t - QS); j < 8 * (t - QS) + 8; ++j) QK(j);
        if constexpr (t == QS + 1) { s0 = tred16(sp, lane); PIN(s0); }
      }
      if constexpr (t == QS + 4) {
        float s1 = tred16(sp + 16, lane);
        s0 = rb(rb(s0) * 0.08838834764831845f);
        s1 = rb(rb(s1) * 0.08838834764831845f);
        mw = wave_max(fmaxf(s0, s1));
        p0 = rb(__expf(s0 - mw)); p1 = rb(__expf(s1 - mw));
        const float lw = wave_sum(p0 + p1);
        const f2 w = {mw, lw};
        sh->ml[wave] = w;
        LDSW();
        *(volatile LAS u32*)&sh->flag[8 + wave] = 1;
        STAMP(5);
      }
      if constexpr (t > QS + 4) {
        constexpr int j0 = pv_done(t - 1), j1 = pv_done(t);
#pragma unroll
        for (int j = j0; j < j1; ++j) PV(j);
      }
      if constexpr (t == landed(WO0 + 7)) frag<4>(wr, WF);
      if constexpr (t == landed(WO0 + 15)) frag<4>(wr + 8, WF + 8);
      if constexpr (t == 48) STAMP(6);
      SB();
    });
    static_assert(landed(WO0 + 15) < 64, "wo arrives too late");
    lds_wait<1>(&sh->flag[4]);
    const f2 g = sh->gml;
    float fw = __expf(mw - g[0]) * g[1];
    PIN(fw);
    constexpr int PD = pv_done(63);
#pragma unroll
    for (int j = PD; j < 32; ++j) { PV(j); SB(); }
    STAMP(7);
    float t4[4];
#pragma unroll
    for (int i = 0; i < 4; ++i) t4[i] = sw32(acc[i], acc[i + 4]);
    sh->ob[wave][4 * (lane & 15) + (lane >> 4)] = pk(sw16(t4[0], t4[2]) * fw, sw16(t4[1], t4[3]) * fw);
    tail<true>(c, sh, rws, WF, gw
    );
  } else {
    const u32 ep = __builtin_amdgcn_raw_buffer_load_b32(rws, O_EPOCH, 0, SC1);
    const int wo_vo = wo_row + 1024 + 32 * (wave - 8) + 4 * (lane & 7);
    u1 wr[16];
    u4 WF[4], gw[8];
#define LD_WO() do { _Pragma("unroll") for (int i = 0; i < 16; ++i) wr[i][0] = __builtin_amdgcn_raw_buffer_load_b32(rwo, wo_vo, (64 * (i >> 3) + (i & 7)) * (D * 2), NT); SB(); } while (0)
    if (wave < 11) {
      const int qt = b < 72 ? b / 18 : (b - 72) / 36, ln = b < 72 ? b - 18 * qt : b - 54 - 36 * qt;
      const int j = wave - 8, r0 = 144 * qt + 48 * j + 6 * (lane >> 3);
      const rsrc_t rc = mkrs(cv, D * 2), rm = mkrs(wmod, b < 216 ? D * 6 * D * 2 : 0);
      const int mo = r0 * (6 * D * 2) + (64 * ln + 8 * (lane & 7)) * 2;
      u4 wm[6];
      const u3 cr = __builtin_amdgcn_raw_buffer_load_b96(rc, r0 * 2, 0, 0);
#pragma unroll
      for (int i = 0; i < 6; ++i) wm[i] = __builtin_amdgcn_raw_buffer_load_b128(rm, mo, i * (6 * D * 2), NT);
      SB();
      bar();
      LD_WO();
      float a[8];
#pragma unroll
      for (int e = 0; e < 8; ++e) a[e] = 0.f;
      u32 act[3];
#pragma unroll
      for (int p = 0; p < 3; ++p) { const u32 w = cr[p]; act[p] = pk(silu(lo16(w)), silu(hi16(w))); PIN(act[p]); }
      SB();
#pragma unroll
      for (int p = 0; p < 3; ++p)
#pragma unroll
        for (int e = 0; e < 4; ++e) { const u32 u = wm[2 * p][e], v = wm[2 * p + 1][e]; a[2 * e] = dot2(plo(u, v), act[p], a[2 * e]); a[2 * e + 1] = dot2(phi(u, v), act[p], a[2 * e + 1]); }
      float t[4], u[2];
#pragma unroll
      for (int i = 0; i < 4; ++i) t[i] = sw32(a[i], a[i + 4]);
#pragma unroll
      for (int i = 0; i < 2; ++i) u[i] = sw16(t[i], t[i + 2]);
      const bool b3 = lane & 8;
      sh->mp[j][8 * (lane & 7) + 2 * (lane >> 4) + ((lane >> 3) & 1)] = (b3 ? u[1] : u[0]) + dpp<ROR8>(b3 ? u[0] : u[1]);
      lds_set(&sh->flag[j], lane);
      STAMP(1);
      if (wave == 8 && b < 216) {
        lds_wait<1>(&sh->flag[1]); lds_wait<1>(&sh->flag[2]);
        const int l = 2 * (lane & 31);
        const f2 m = *(const LAS f2*)&sh->mp[0][l] + *(const LAS f2*)&sh->mp[1][l] + *(const LAS f2*)&sh->mp[2][l];
        const float m0 = m[0], m1 = m[1];
        const u4 o = {asu(m0), ep + 1, asu(m1), ep + 1};
        if (lane < 32) st128(o, rws, O_MOD + qt * MOD_STR + ln * 512 + lane * 16);
        STAMP(2);
      }
    } else {
      const rsrc_t rx = mkrs(x, D * 2), rq = mkrs(wq, D * 4096 * 2);
      const int g = lane >> 4, pc = (lane & 15) < 9 ? (lane & 15) : 8, pidx = 36 * s + 9 * g + pc;
      const u4 xa = __builtin_amdgcn_raw_buffer_load_b128(rx, lane * 16, 0, 0);
      const u4 xb = __builtin_amdgcn_raw_buffer_load_b128(rx, 1024 + lane * 16, 0, 0);
      const u32 xp = __builtin_amdgcn_raw_buffer_load_b32(rx, pidx * 4, 0, 0);
      if (lane < 16) { sh->flag[lane] = 0; f2 z = {0.f, 0.f}; sh->st[lane] = z; }
      { f2 z = {0.f, 0.f}; *(LAS f2*)&sh->gp[lane][18] = z; }
      SB();
      bar();
      u4 wv[18];
      const int qo = (72 * s + 18 * g) * 8192 + (128 * h + 8 * (lane & 15)) * 2;
#pragma unroll
      for (int i = 0; i < 18; ++i) wv[i] = __builtin_amdgcn_raw_buffer_load_b128(rq, qo, i * 8192, NT);
      SB();
      LD_WO();
      const u32 tag = ep + 1;
      sh->tag = tag;
      float l0, l1;
      {
        float sum = 0.f;
#pragma unroll
        for (int i = 0; i < 4; ++i) { const u32 u = xa[i], v = xb[i]; sum += (lo16(u) + hi16(u)) + (lo16(v) + hi16(v)); }
        const float mean = wave_sum(sum) * (1.f / D);
        float sa = 0.f, sb = 0.f;
#pragma unroll
        for (int i = 0; i < 4; ++i) {
          const u32 u = xa[i], v = xb[i];
          const float d0 = lo16(u) - mean, d1 = hi16(u) - mean, d2 = lo16(v) - mean, d3 = hi16(v) - mean;
          sa = fmaf(d0, d0, fmaf(d1, d1, sa)); sb = fmaf(d2, d2, fmaf(d3, d3, sb));
        }
        const float rs = ln_rstd(wave_sum(lane < 8 ? sa + sb : sa) * (1.f / D));
        l0 = rb(rb(lo16(xp) - rb(mean)) * rs); l1 = rb(rb(hi16(xp) - rb(mean)) * rs);
        PIN(l0); PIN(l1);
      }
      u32 P[9][8];
#pragma unroll
      for (int p = 0; p < 9; ++p)
#pragma unroll
        for (int e = 0; e < 4; ++e) { const u32 u = wv[2 * p][e], v = wv[2 * p + 1][e]; P[p][2 * e] = plo(u, v); P[p][2 * e + 1] = phi(u, v); PIN(P[p][2 * e]); PIN(P[p][2 * e + 1]); }
      STAMP(1);
      {
        int o = O_MOD + pidx * 16;
        u4 v[8];
#pragma nounroll
        for (u32 n = 0; n < SPIN_G; ++n) {
          FENCE(); PIN(o);
          u32 bad = 0;
#pragma unroll
          for (int i = 0; i < 8; ++i) v[i] = ld128(rws, o + (i >> 1) * MOD_STR + (i & 1) * 4608);
#pragma unroll
          for (int i = 0; i < 8; ++i) bad |= (v[i][1] ^ tag) | (v[i][3] ^ tag);
          if (__all(bad == 0)) break;
        }
        STAMP(2);
        float m[4];
#pragma unroll
        for (int i = 0; i < 4; ++i) {
          const u32 e0 = v[i >> 1][2 * (i & 1)], e1 = v[2 + (i >> 1)][2 * (i & 1)], e2 = v[4 + (i >> 1)][2 * (i & 1)], e3 = v[6 + (i >> 1)][2 * (i & 1)];
          m[i] = rb((asf(e0) + asf(e1)) + (asf(e2) + asf(e3)));
        }
        const u32 hp = pk(rb(l0 * rb(1.f + m[2])) + m[0], rb(l1 * rb(1.f + m[3])) + m[1]);
        float a[8];
#pragma unroll
        for (int e = 0; e < 8; ++e) a[e] = 0.f;
#pragma unroll
        for (int p = 0; p < 9; ++p) {
          const u32 hb = nbc(hp, p);
#pragma unroll
          for (int e = 0; e < 8; ++e) a[e] = dot2(P[p][e], hb, a[e]);
        }
        float t[4];
#pragma unroll
        for (int i = 0; i < 4; ++i) t[i] = sw32(a[i], a[i + 4]);
        const u4 qo4 = {asu(sw16(t[0], t[2])), tag, asu(sw16(t[1], t[3])), tag};
        st128(qo4, rws, O_QP + c.pi * 1024 + lane * 16);
      }
      {
        int o = O_QP + h * 8192 + lane * 16;
        u4 v[8];
#pragma nounroll
        for (u32 n = 0; n < SPIN_G; ++n) {
          FENCE(); PIN(o);
          u32 bad = 0;
#pragma unroll
          for (int i = 0; i < 8; ++i) v[i] = ld128(rws, o + i * 1024);
#pragma unroll
          for (int i = 0; i < 8; ++i) bad |= (v[i][1] ^ tag) | (v[i][3] ^ tag);
          if (__all(bad == 0)) break;
        }
        float q0 = 0.f, q1 = 0.f;
#pragma unroll
        for (int i = 0; i < 8; ++i) { const u32 u = v[i][0], w = v[i][2]; q0 += asf(u); q1 += asf(w); }
        sh->q[4 * (lane & 15) + g] = pk(q0, q1);
        lds_set(&sh->flag[3], lane);
      }
      STAMP(3);
      const bool red = b >= 64 && b < 100;
      if (red) {
        const int pr = 8 * (b - 64) + (lane & 7), vec = (lane >> 3) & 3;
        const u32 xq = __builtin_amdgcn_raw_buffer_load_b32(rx, pr * 4, 0, 0);
        int o = O_MOD + (lane >> 5) * 2 * MOD_STR + (2 + vec) * 4608 + pr * 16;
        u4 v0, v1;
#pragma nounroll
        for (u32 n = 0; n < SPIN_G; ++n) {
          FENCE(); PIN(o);
          v0 = ld128(rws, o); v1 = ld128(rws, o + MOD_STR);
          if (__all(((v0[1] ^ tag) | (v0[3] ^ tag) | (v1[1] ^ tag) | (v1[3] ^ tag)) == 0)) break;
        }
        const u32 a0 = v0[0], a1 = v0[2], b0 = v1[0], b1 = v1[2];
        const float m0 = asf(a0) + asf(b0), m1 = asf(a1) + asf(b1);
        const u32 d = pk(sw32(m0, m0), sw32(m1, m1));
        const u4 o4 = {d, tag, dppu<SHL + 1>(d), tag};
        if (lane >= 8 && lane < 24 && !(lane & 1)) st128(o4, rws, O_SC + ((vec - 1) * 288 + pr) * 8);
        if (lane < 32) sh->sc[lane] = vec == 1 ? xq : d;
      }
      {
#pragma nounroll
        for (u32 n = 0; n < SPIN_L; ++n) {
          if (__all(((volatile LAS u32*)sh->flag)[8 + (lane & 7)] != 0)) break;
          __builtin_amdgcn_s_sleep(4);
        }
        FENCE();
        const f2 m = sh->ml[lane & 7];
        const float M = max8(m[0]);
        const float L = sum8(m[1] * __expf(m[0] - M));
        const u4 o = {asu(M), tag, asu(L), tag};
        if (lane == 0) st128(o, rws, O_ML + c.pi * 16);
        const f2 w = {M, 1.f / L};
        sh->gml = w;
        lds_set(&sh->flag[4], lane);
      }
      STAMP(4);
      if (ffn) {
        int o = O_SC + lane * 16;
        u4 v[5];
#pragma nounroll
        for (u32 n = 0; n < SPIN_G; ++n) {
          FENCE(); PIN(o);
          u32 bad = 0;
#pragma unroll
          for (int i = 0; i < 5; ++i) v[i] = ld128(rws, o + (i < 4 ? i * 1024 : 3584));
#pragma unroll
          for (int i = 0; i < 5; ++i) bad |= (v[i][1] ^ tag) | (v[i][3] ^ tag);
          if (__all(bad == 0)) break;
          __builtin_amdgcn_s_sleep(16);
        }
#pragma unroll
        for (int i = 0; i < 5; ++i) { const u2 w = {v[i][0], v[i][2]}; *(LAS u2*)&sh->sc[(i < 4 ? 128 * i : 448) + 2 * lane] = w; }
      } else if (red) {
        int o = O_ML + lane * 16;
        u4 v[4];
#pragma nounroll
        for (u32 n = 0; n < SPIN_G; ++n) {
          FENCE(); PIN(o);
          u32 bad = 0;
#pragma unroll
          for (int i = 0; i < 4; ++i) v[i] = ld128(rws, o + i * 1024);
#pragma unroll
          for (int i = 0; i < 4; ++i) bad |= (v[i][1] ^ tag) | (v[i][3] ^ tag);
          if (__all(bad == 0)) break;
          __builtin_amdgcn_s_sleep(8);
        }
#pragma unroll
        for (int i = 0; i < 4; ++i) {
          const u32 mu = v[i][0], lu = v[i][2];
          const float e = asf(lu) * __expf(asf(mu) - max8(asf(mu)));
          sh->wt[64 * i + lane] = e / sum8(e);
        }
      }
      STAMP(6);
    }
    frag<1>(wr, WF);
    frag<1>(wr + 8, WF + 2);
    SB();
    if (wave < 11) lds_wait<8>(&sh->flag[4]);
    STAMP(7);
    tail<false>(c, sh, rws, WF, gw
    );
  }
}

extern "C" void kernel_launch(void* const* d_in, const int* in_sizes, int n_in, void* d_out, int out_size, void* d_ws, size_t ws_size,
                              hipStream_t stream) {
  fused_kernel<<<256, 768, 0, stream>>>((const u16*)d_in[0], (const u16*)d_in[1], (const u16*)d_in[2], (const u16*)d_in[3],
                                        (const u16*)d_in[4], (const u16*)d_in[6], (const u16*)d_in[7], (const u16*)d_in[8],
                                        (const u16*)d_in[9], (const u16*)d_in[10], (u16*)d_out, (char*)d_ws);
}
```

```cpp
#define STAMPS 1
#include <hip/hip_runtime.h>
#include <stdint.h>

#define WK 3
#define WK2 3
#define T2 48
#define PV0 4
#define WO0 41
#define GN 0
#define RX 16
#define YSTR 128

typedef unsigned u32;
typedef unsigned long long u64;
typedef unsigned short u16;
typedef u32 u1 __attribute__((ext_vector_type(1)));
typedef u32 u2 __attribute__((ext_vector_type(2)));
typedef u32 u3 __attribute__((ext_vector_type(3)));
typedef u32 u4 __attribute__((ext_vector_type(4)));
typedef float f2 __attribute__((ext_vector_type(2)));
typedef float f4 __attribute__((ext_vector_type(4)));
typedef __bf16 bf2 __attribute__((ext_vector_type(2)));
typedef __bf16 bf8 __attribute__((ext_vector_type(8)));
typedef __amdgpu_buffer_rsrc_t rsrc_t;
#define LAS __attribute__((address_space(3)))
#define GAS __attribute__((address_space(1)))
#define DI __device__ __forceinline__

constexpr int D = 576, FF = 4608;
constexpr int SC1 = 16, NT = 2;
constexpr u32 SPIN_G = 1u << 17, SPIN_L = 1u << 20;

constexpr int O_EPOCH = 0;
constexpr int O_SC = 4096;
constexpr int O_ML = 12288;
constexpr int O_MOD = 16384, MOD_STR = 3456 * 8;
constexpr int O_X2 = 131072;
constexpr int O_QP = 262144;
constexpr int O_YA = O_QP + 262144;
constexpr int O_FC = O_YA + 36 * 256 * YSTR;
constexpr int O_END = O_FC + 36 * 144 * YSTR;
constexpr int O_STAMP = 4 << 20;

DI float asf(u32 x) { return __uint_as_float(x); }
DI u32 asu(float x) { return __float_as_uint(x); }
DI u32 pk(float lo, float hi) { f2 t = {lo, hi}; bf2 r = __builtin_convertvector(t, bf2); return __builtin_bit_cast(u32, r); }
DI float rb(float x) { return asf(pk(0.f, x)); }
DI float lo16(u32 x) { return asf(x << 16); }
DI float hi16(u32 x) { return asf(x & 0xffff0000u); }
template <int C> DI u32 dppu(u32 x) { return (u32)__builtin_amdgcn_update_dpp(0, (int)x, C, 0xf, 0xf, true); }
template <int C> DI float dpp(float x) { return asf(dppu<C>(asu(x))); }
constexpr int ROR4 = 0x124, ROR8 = 0x128, HMIR = 0x141, QX1 = 0xB1, QX2 = 0x4E, SHL = 0x100, NBC = 0x150;
DI u32 nbc(u32 src, int n) {
  switch (n) {
#define CASE(n) case n: return dppu<NBC + n>(src);
    CASE(0) CASE(1) CASE(2) CASE(3) CASE(4) CASE(5) CASE(6) CASE(7) CASE(8) CASE(9) CASE(10) CASE(11) CASE(12) CASE(13) CASE(14)
#undef CASE
    default: return dppu<NBC + 15>(src);
  }
}
DI float dot2(u32 a, u32 b, float c) { return __builtin_amdgcn_fdot2_f32_bf16(__builtin_bit_cast(bf2, a), __builtin_bit_cast(bf2, b), c, false); }
DI float sw32(float a, float b) { auto r = __builtin_amdgcn_permlane32_swap(asu(a), asu(b), false, false); u32 r0 = r[0], r1 = r[1]; return asf(r0) + asf(r1); }
DI float sw16(float a, float b) { auto r = __builtin_amdgcn_permlane16_swap(asu(a), asu(b), false, false); u32 r0 = r[0], r1 = r[1]; return asf(r0) + asf(r1); }
DI float mx32(float a) { auto r = __builtin_amdgcn_permlane32_swap(asu(a), asu(a), false, false); u32 r0 = r[0], r1 = r[1]; return fmaxf(asf(r0), asf(r1)); }
DI float mx16(float a) { auto r = __builtin_amdgcn_permlane16_swap(asu(a), asu(a), false, false); u32 r0 = r[0], r1 = r[1]; return fmaxf(asf(r0), asf(r1)); }
DI float row_sum(float v) { v += dpp<QX1>(v); v += dpp<QX2>(v); v += dpp<ROR4>(v); return v + dpp<ROR8>(v); }
DI float wave_sum(float v) { v = row_sum(v); v = sw16(v, v); return sw32(v, v); }
DI float wave_max(float v) {
  v = fmaxf(v, dpp<QX1>(v)); v = fmaxf(v, dpp<QX2>(v)); v = fmaxf(v, dpp<ROR4>(v)); v = fmaxf(v, dpp<ROR8>(v));
  return mx32(mx16(v));
}
DI float sum8(float v) { v += dpp<QX1>(v); v += dpp<QX2>(v); return v + dpp<HMIR>(v); }
DI float max8(float v) { v = fmaxf(v, dpp<QX1>(v)); v = fmaxf(v, dpp<QX2>(v)); return fmaxf(v, dpp<HMIR>(v)); }
DI u32 plo(u32 a, u32 b) { return __builtin_amdgcn_perm(b, a, 0x05040100u); }
DI u32 phi(u32 a, u32 b) { return __builtin_amdgcn_perm(b, a, 0x07060302u); }
DI f4 mfma(u4 a, u4 b, f4 c) { return __builtin_amdgcn_mfma_f32_16x16x32_bf16(__builtin_bit_cast(bf8, a), __builtin_bit_cast(bf8, b), c, 0, 0, 0); }
DI rsrc_t mkrs(const void* p, u32 n) { return __builtin_amdgcn_make_buffer_rsrc((void*)p, 0, n, 0x00020000); }
#define FENCE() asm volatile("" ::: "memory")
#define SB() __builtin_amdgcn_sched_barrier(0)
#define PIN(v) asm volatile("" : "+v"(v))
#define VMCNT(n) asm volatile("s_waitcnt vmcnt(%0)" ::"n"(n) : "memory")
#define LDSW() asm volatile("s_waitcnt lgkmcnt(0)" ::: "memory")
DI void bar() { asm volatile("s_waitcnt lgkmcnt(0)\n\ts_barrier" ::: "memory"); }
DI float silu(float v) { return v * __builtin_amdgcn_rcpf(1.f + __expf(-v)); }
DI u4 ld128(rsrc_t r, int o) { return __builtin_amdgcn_raw_buffer_load_b128(r, o, 0, SC1); }
DI u2 ld64(rsrc_t r, int o) { return __builtin_amdgcn_raw_buffer_load_b64(r, o, 0, SC1); }
DI void st128(u4 v, rsrc_t r, int o) { __builtin_amdgcn_raw_buffer_store_b128(v, r, o, 0, SC1); }
DI float ln_rstd(float var) { return __builtin_amdgcn_rcpf(rb(__builtin_amdgcn_sqrtf(rb(rb(var) + 1e-5f)))); }

struct __attribute__((aligned(16))) Sh {
  float gp[64][20];
  float red[16][12];
  float red2[16][12];
  float mp[3][64];
  float wt[256];
  u32 sc[576];
  u32 ob[8][72];
  u32 q[64];
  f2 st[16];
  f2 ml[8];
  f2 gml;
  u32 tag;
  u32 pad;
  u32 flag[16];
};

#define STAMP(i) do { const u32 t_ = (u32)__builtin_amdgcn_s_memrealtime(); asm volatile("v_writelane_b32 %0, %1, %2" : "+v"(ts) : "s"(t_), "n"(i)); } while (0)
#define STAMP_OUT() do { const u32 t0_ = __builtin_amdgcn_readfirstlane(*(volatile u32*)(ws + (8 << 20))); const u32 lim_ = 6000u; for (u32 n_ = 0; n_ < 100000u; ++n_) { if ((u32)__builtin_amdgcn_s_memrealtime() - t0_ >= lim_) break; __builtin_amdgcn_s_sleep(1); } } while (0)

template <int S> DI void lds_wait(volatile LAS u32* f) {
#pragma nounroll
  for (u32 n = 0; n < SPIN_L; ++n) { if (*f != 0) break; __builtin_amdgcn_s_sleep(S); }
  FENCE();
}
DI void lds_set(LAS u32* f, int lane) { LDSW(); if (lane == 0) *(volatile LAS u32*)f = 1; }

DI float tred16(const float* s, int lane) {
  float t[8], u[4], v[2];
  const bool b3 = lane & 8, b2 = lane & 4, b1 = lane & 2, b0 = lane & 1;
#pragma unroll
  for (int i = 0; i < 8; ++i) t[i] = (b3 ? s[i + 8] : s[i]) + dpp<ROR8>(b3 ? s[i] : s[i + 8]);
#pragma unroll
  for (int i = 0; i < 4; ++i) u[i] = (b2 ? t[i + 4] : t[i]) + dpp<HMIR>(b2 ? t[i] : t[i + 4]);
#pragma unroll
  for (int i = 0; i < 2; ++i) v[i] = (b1 ? u[i + 2] : u[i]) + dpp<QX2>(b1 ? u[i] : u[i + 2]);
  return (b0 ? v[1] : v[0]) + dpp<QX1>(b0 ? v[0] : v[1]);
}

template <int C, typename V> DI void frag(const V* L, u4* F) {
#pragma unroll
  for (int c = 0; c < C; ++c) {
    u4 a, b;
#pragma unroll
    for (int j = 0; j < 4; ++j) { const u32 x = L[2 * j][c], y = L[2 * j + 1][c]; a[j] = plo(x, y); b[j] = phi(x, y); }
    PIN(a); PIN(b);
    F[2 * c] = a; F[2 * c + 1] = b;
  }
}

DI u4 gather8(float v) {
  const u32 d = pk(v, dpp<SHL + 1>(v));
  u4 a; a[0] = d; a[1] = dppu<SHL + 2>(d); a[2] = dppu<SHL + 4>(d); a[3] = dppu<SHL + 6>(d);
  return a;
}

template <int I> struct IC { static constexpr int value = I; };
template <int I, int N, class F> DI void sfor(F&& f) { if constexpr (I < N) { f(IC<I>{}); sfor<I + 1, N>(f); } }

struct Ctx {
  const u16* wd; u16* out; char* ws; const char* gup;
  int b, pi, lane, wave;
};
DI void swap16(u32 d, u32& r0, u32& r1) { auto r = __builtin_amdgcn_permlane16_swap(d, d, false, false); r0 = r[0]; r1 = r[1]; }
DI void swap32(u32 d, u32& r0, u32& r1) { auto r = __builtin_amdgcn_permlane32_swap(d, d, false, false); r0 = r[0]; r1 = r[1]; }
#define LD_GU(p, j) __builtin_nontemporal_load((const GAS u4*)((p) + (j) * (FF * 2)))

template <bool STREAM>
DI void tail(const Ctx& c, LAS Sh* sh, rsrc_t rws, const u4* WF, u4* gw
             , u32 ts
) {
  const int lane = c.lane, wave = c.wave;
  char* ws = c.ws; (void)ws;
  const u32 tag = sh->tag;
  int yo = O_YA + c.pi * YSTR + (STREAM ? (4 * wave + ((lane & 7) >> 1)) * 256 * YSTR + (2 * (lane & 1) + (lane >> 5)) * 16
                                        : (24 + wave) * 256 * YSTR + (lane >> 1) * 16);
  PIN(yo);
  bar();
  STAMP(8);
  {
    constexpr int NTL = STREAM ? 8 : 2;
    const LAS u32* ob = &sh->ob[(lane & 15) >> 1][8 * (lane >> 4) + 4 * (lane & 1)];
    const u4 a0 = *(const LAS u4*)ob, a1 = *(const LAS u4*)(ob + 32);
    f4 dd[NTL];
#pragma unroll
    for (int j = 0; j < NTL; ++j) { f4 z = {0.f, 0.f, 0.f, 0.f}; dd[j] = mfma(a0, WF[j], z); }
#pragma unroll
    for (int j = 0; j < NTL; ++j) dd[j] = mfma(a1, WF[NTL + j], dd[j]);
    const bool half = lane & 8;
    float v[NTL];
#pragma unroll
    for (int j = 0; j < NTL; ++j) v[j] = half ? dd[j][1] + dd[j][3] : dd[j][0] + dd[j][2];
    u32 r0, r1;
    if constexpr (STREAM) {
      float t[4], u[2];
#pragma unroll
      for (int i = 0; i < 4; ++i) t[i] = sw32(v[i], v[i + 4]);
#pragma unroll
      for (int i = 0; i < 2; ++i) { u[i] = sw16(t[i], t[i + 2]); u[i] += dpp<ROR8>(u[i]); }
      swap16(pk(u[0], u[1]), r0, r1);
      const u4 o = {r0, tag, r1, tag};
      if ((lane & 24) == 0) st128(o, rws, yo);
    } else {
      float t = sw32(v[0], v[1]);
      t = sw16(t, t); t += dpp<ROR8>(t);
      swap32(asu(t), r0, r1);
      const u32 d = pk(asf(r0), asf(r1));
      const u4 o = {d, tag, dppu<SHL + 1>(d), tag};
      if ((lane & 57) == 0) st128(o, rws, yo);
    }
  }
  STAMP(9);
  if (c.b >= 112) {
    const int f = c.b - 112;
    bar();
    if (STREAM || wave == 8) {
#pragma unroll
      for (int j = STREAM ? GN : 0; j < 8; ++j) gw[j] = LD_GU(c.gup, j);
      SB();
      int o1 = O_X2 + (f % RX) * 4608 + (4 * wave + (lane >> 4)) * 128 + ((lane & 15) >> 1) * 8;
      const float sbv = lo16(((const LAS u16*)sh->sc)[64 * wave + lane]);
      float cbv = rb(1.f + lo16(((const LAS u16*)sh->sc)[D + 64 * wave + lane]));
      PIN(cbv);
      u4 GF[8];
      frag<4>(gw, GF);
      SB();
      STAMP(10);
      u2 xr;
#pragma nounroll
      for (u32 n = 0; n < SPIN_G; ++n) {
        FENCE(); PIN(o1);
        xr = ld64(rws, o1);
        if (__all(xr[1] == tag)) break;
      }
      STAMP(11);
      const u32 x0 = xr[0];
      const float xv = (lane & 1) ? hi16(x0) : lo16(x0);
      { const f2 w = {wave_sum(xv), wave_sum(xv * xv)}; sh->st[wave] = w; }
      bar();
      const f2 st = sh->st[lane & 15];
      const float mean = row_sum(st[0]) * (1.f / D), e2 = row_sum(st[1]) * (1.f / D);
      const float rs = __builtin_amdgcn_rsqf(fmaxf(fmaf(-mean, mean, e2), 0.f) + 1e-5f);
      const u4 a = gather8(fmaf(xv - mean, rs * cbv, sbv));
      f4 dd[8];
#pragma unroll
      for (int j = 0; j < 8; ++j) { f4 z = {0.f, 0.f, 0.f, 0.f}; dd[j] = mfma(a, GF[j], z); }
      const int par = (lane >> 2) & 1;
      if ((lane >> 4) == 2 * par) {
        LAS float* gp = &sh->gp[32 * ((lane >> 3) & 1) + 8 * (lane & 3)][2 * wave + par];
#pragma unroll
        for (int j = 0; j < 8; ++j) gp[20 * j] = dd[j][0];
      }
      bar();
      STAMP(12);
    } else if (!STREAM) {
      const int dw = wave - 9, g = lane >> 4;
      const rsrc_t rwd = mkrs(c.wd + (size_t)(576 * (c.b & 7) + 32 * (f >> 3)) * D, 32 * D * 2);
      u2 wd[3][8];
      const int vo = 8 * g * (D * 2) + (192 * dw + 4 * (lane & 15)) * 2;
      __builtin_amdgcn_s_sleep(4);
#pragma unroll
      for (int j = 0; j < 8; ++j)
#pragma unroll
        for (int e = 0; e < 3; ++e) wd[e][j] = __builtin_amdgcn_raw_buffer_load_b64(rwd, vo + e * 128, j * (D * 2), NT);
      SB();
      int fo = O_FC + ((12 * dw + ((lane & 15) >> 2)) * 144 + f) * YSTR + (lane & 3) * 16;
      PIN(fo);
      u4 DF[12];
#pragma unroll
      for (int e = 0; e < 3; ++e) frag<2>(wd[e], DF + 4 * e);
      SB();
      STAMP(10);
      bar();
      bar();
      STAMP(12);
      const LAS float* gp = sh->gp[32 * ((lane >> 3) & 1) + 8 * g + (lane & 7)];
      f4 t[5];
#pragma unroll
      for (int i = 0; i < 5; ++i) t[i] = *(const LAS f4*)(gp + 4 * i);
      const f4 tt = (t[0] + t[1]) + (t[2] + t[3]) + t[4];
      const float gs = (tt[0] + tt[1]) + (tt[2] + tt[3]);
      const float us = dpp<SHL + 8>(gs);
      const u4 a = gather8(silu(gs) * us);
#pragma unroll
      for (int e = 0; e < 3; ++e) {
        float r[4];
#pragma unroll
        for (int j = 0; j < 4; ++j) { f4 z = {0.f, 0.f, 0.f, 0.f}; const f4 t4 = mfma(a, DF[4 * e + j], z); r[j] = t4[0]; }
        const u4 o = {pk(r[0], r[1]), tag, pk(r[2], r[3]), tag};
        if (lane < 16) st128(o, rws, fo + e * 4 * 144 * YSTR);
      }
      STAMP(13);
    }
  } else if (c.b >= 64 && c.b < 100) {
    const int rho = c.b - 64;
    const int l16 = lane & 15;
    float xin = 0.f, ga = 0.f, gb = 0.f;
    if (STREAM) {
      if (wave < RX / 4) { ga = lo16(((const LAS u16*)sh->sc)[l16]); xin = lo16(((const LAS u16*)sh->sc)[16 + l16]); gb = lo16(((const LAS u16*)sh->sc)[48 + l16]); }
      const float w0 = sh->wt[32 * wave + (lane >> 2)], w1 = sh->wt[32 * wave + 16 + (lane >> 2)];
      int o = O_YA + (rho * 256 + 32 * wave + (lane >> 2)) * YSTR + (lane & 3) * 16;
      u4 v0 = {0u, ~tag, 0u, ~tag}, v1 = v0;
#pragma nounroll
      for (u32 n = 0; n < SPIN_G; ++n) {
        const int oa = (v0[1] == tag) & (v0[3] == tag) ? O_END : o, ob = (v1[1] == tag) & (v1[3] == tag) ? O_END : o + 16 * YSTR;
        if (__all((oa == O_END) & (ob == O_END))) break;
        FENCE();
        const u4 n0 = ld128(rws, oa), n1 = ld128(rws, ob);
        v0 = oa == O_END ? v0 : n0; v1 = ob == O_END ? v1 : n1;
      }
      STAMP(10);
      const u32 a0 = v0[0], a2 = v0[2], b0 = v1[0], b2 = v1[2];
      float y[4] = {fmaf(lo16(a0), w0, lo16(b0) * w1), fmaf(hi16(a0), w0, hi16(b0) * w1), fmaf(lo16(a2), w0, lo16(b2) * w1), fmaf(hi16(a2), w0, hi16(b2) * w1)};
      float r = sw16(sw32(y[0], y[2]), sw32(y[1], y[3]));
      r += dpp<ROR4>(r); r += dpp<ROR8>(r);
      if ((lane & 12) == 0) sh->red[4 * (lane & 3) + (lane >> 4)][wave] = r;
    }
    bar();
    float x2 = 0.f;
    if (STREAM && wave < RX / 4) {
      const LAS float* rp = sh->red[l16];
      const f4 tt = *(const LAS f4*)rp + *(const LAS f4*)(rp + 4);
      x2 = rb(fmaf(ga, (tt[0] + tt[1]) + (tt[2] + tt[3]), xin));
      const u32 d = phi(asu(x2), dppu<SHL + 1>(asu(x2)));
      const u4 o = {d, tag, dppu<SHL + 2>(d), tag};
      if ((lane & 3) == 0) st128(o, rws, O_X2 + (4 * wave + (lane >> 4)) * 4608 + rho * 128 + l16 * 4);
      STAMP(11);
    }
    if (STREAM && wave == 7 && rho == 0 && lane == 0) __builtin_amdgcn_raw_buffer_store_b32(tag, rws, O_EPOCH, 0, SC1);
    if (STREAM || wave == 8) {
      int o = O_FC + (rho * 144 + 16 * wave + (lane >> 2)) * YSTR + (lane & 3) * 16;
      u4 v;
#pragma nounroll
      for (u32 n = 0; n < SPIN_G; ++n) {
        FENCE(); PIN(o);
        v = ld128(rws, o);
        if (__all((v[1] == tag) & (v[3] == tag))) break;
      }
      STAMP(12);
      const u32 a0 = v[0], a2 = v[2];
      float r = sw16(sw32(lo16(a0), lo16(a2)), sw32(hi16(a0), hi16(a2)));
      r += dpp<ROR4>(r); r += dpp<ROR8>(r);
      if ((lane & 12) == 0) sh->red2[4 * (lane & 3) + (lane >> 4)][wave] = r;
    }
    bar();
    if (STREAM && wave == 0) {
      const LAS float* rp = sh->red2[l16];
      const f4 tt = *(const LAS f4*)rp + *(const LAS f4*)(rp + 4);
      const float r0 = fmaf(gb, ((tt[0] + tt[1]) + (tt[2] + tt[3])) + rp[8], x2);
      if (lane < 16) c.out[16 * rho + lane] = (u16)pk(r0, 0.f);
      STAMP(13);
    }
  }
  STAMP(15);
  STAMP_OUT();
}

constexpr int win(int t) { return t < T2 ? WK : (WK + t - T2 + 1 < WK2 ? WK + t - T2 + 1 : WK2); }
constexpr int extras_after(int t) { return ((t >= WO0 && t < WO0 + 16) ? 1 : 0) + (t >= 64 - GN ? 1 : 0); }
constexpr int issued_before(int t) { int n = 0; for (int i = 0; i < t; ++i) n += 1 + extras_after(i); return n; }
constexpr int thr(int t) { return issued_before(t) - issued_before(t - win(t)) - 1; }
constexpr int QS = 32 + PV0 - 1;
constexpr int pv_done(int t) {
  int n = 0;
  for (int i = QS + 5; i <= t; ++i) { const int av = i - 32 - win(i) + 1; n = n + 3 < av ? n + 3 : (av > n ? av : n); }
  return n;
}
constexpr int landed(int t) { int i = t + 1; while (i < 64 && i - win(i) <= t) ++i; return i; }

__global__ void __launch_bounds__(768, 3)
fused_kernel(const u16* __restrict__ x, const u16* __restrict__ kc, const u16* __restrict__ vc, const u16* __restrict__ wq,
             const u16* __restrict__ wo, const u16* __restrict__ wg, const u16* __restrict__ wu, const u16* __restrict__ wd,
             const u16* __restrict__ cv, const u16* __restrict__ wmod, u16* __restrict__ out, char* ws) {
  __shared__ Sh shm;
  LAS Sh* sh = (LAS Sh*)&shm;
  u32 ts = 0;
  STAMP(0);
  const int lane = threadIdx.x & 63, wave = __builtin_amdgcn_readfirstlane(threadIdx.x >> 6);
  const int b = blockIdx.x, xc = b & 7, ci = b >> 3, s = ci & 7, h = (ci >> 3) * 8 + xc;
  const bool ffn = b >= 112;
  Ctx c; c.wd = wd; c.out = out; c.ws = ws; c.b = b; c.pi = 8 * h + s; c.lane = lane; c.wave = wave;
  const rsrc_t rws = mkrs(ws, O_END);
  const rsrc_t rwo = mkrs(wo + (size_t)h * 128 * D, 128 * D * 2);
  const int wo_row = (16 * (lane >> 4) + (lane & 8)) * (D * 2);
  c.gup = (const char*)(((lane & 8) ? wu : wg) + (size_t)(64 * (wave < 8 ? wave : 8) + 16 * (lane >> 4) + 2 * (lane & 4)) * FF + 576 * xc + 32 * (ffn ? ci - 14 : 0) + 8 * (lane & 3));

  if (wave < 8) {
    const size_t slab = ((size_t)h * 8192 + s * 1024) * 128;
    const rsrc_t rk = mkrs(kc + slab, 262144), rv = mkrs(vc + slab, 262144);
    const int vo = (wave >> 2) * 131072 + (wave & 3) * 1024 + lane * 16;
    const int wo_vo = wo_row + 128 * wave + 16 * (lane & 7);
    u4 kb[32], vb[32], qv, gw[8], WF[16], wr[16];
    float sp[32], s0 = 0.f, mw = 0.f, p0 = 0.f, p1 = 0.f, acc[8];
#pragma unroll
    for (int i = 0; i < 8; ++i) acc[i] = 0.f;
#define QK(i) do { float a_ = dot2(kb[i][0], qv[0], 0.f), b_ = dot2(kb[i][1], qv[1], 0.f); a_ = dot2(kb[i][2], qv[2], a_); b_ = dot2(kb[i][3], qv[3], b_); sp[i] = a_ + b_; PIN(sp[i]); } while (0)
#define PV(i) do { const u32 pb_ = nbc(asu((i) < 16 ? p0 : p1), (i) & 15), pl_ = pb_ >> 16; \
      _Pragma("unroll") for (int cc = 0; cc < 4; ++cc) { const u32 w_ = vb[i][cc]; acc[2 * cc] = dot2(w_, pl_, acc[2 * cc]); acc[2 * cc + 1] = dot2(w_, pb_, acc[2 * cc + 1]); } \
      _Pragma("unroll") for (int cc = 0; cc < 8; ++cc) PIN(acc[cc]); } while (0)
    sfor<0, 64>([&](auto T) __attribute__((always_inline)) {
      constexpr int t = T.value;
      if constexpr (t >= WK) VMCNT(thr(t));
      if constexpr (t == WK) STAMP(1);
      if constexpr (t < 32) kb[t] = __builtin_amdgcn_raw_buffer_load_b128(rk, vo, t * 4096, NT);
      else vb[t - 32] = __builtin_amdgcn_raw_buffer_load_b128(rv, vo, (t - 32) * 4096, NT);
      if constexpr (t >= WO0 && t < WO0 + 16) wr[t - WO0] = __builtin_amdgcn_raw_buffer_load_b128(rwo, wo_vo, (64 * ((t - WO0) >> 3) + ((t - WO0) & 7)) * (D * 2), NT);
      if constexpr (t >= 64 - GN) gw[t - 64 + GN] = LD_GU(c.gup, t - 64 + GN);
      SB();
      if constexpr (t == WK - 1) bar();
      if constexpr (t == QS) {
        STAMP(2);
        lds_wait<1>(&sh->flag[3]);
        STAMP(3);
        qv = *(const LAS u4*)(sh->q + 4 * (lane & 15));
      }
      if constexpr (t >= QS && t < QS + 4) {
#pragma unroll
        for (int j = 8 * (t - QS); j < 8 * (t - QS) + 8; ++j) QK(j);
        if constexpr (t == QS + 1) { s0 = tred16(sp, lane); PIN(s0); }
      }
      if constexpr (t == QS + 4) {
        float s1 = tred16(sp + 16, lane);
        s0 = rb(rb(s0) * 0.08838834764831845f);
        s1 = rb(rb(s1) * 0.08838834764831845f);
        mw = wave_max(fmaxf(s0, s1));
        p0 = rb(__expf(s0 - mw)); p1 = rb(__expf(s1 - mw));
        const float lw = wave_sum(p0 + p1);
        const f2 w = {mw, lw};
        sh->ml[wave] = w;
        LDSW();
        *(volatile LAS u32*)&sh->flag[8 + wave] = 1;
        STAMP(5);
      }
      if constexpr (t > QS + 4) {
        constexpr int j0 = pv_done(t - 1), j1 = pv_done(t);
#pragma unroll
        for (int j = j0; j < j1; ++j) PV(j);
      }
      if constexpr (t == landed(WO0 + 7)) frag<4>(wr, WF);
      if constexpr (t == landed(WO0 + 15)) frag<4>(wr + 8, WF + 8);
      if constexpr (t == 48) STAMP(6);
      SB();
    });
    static_assert(landed(WO0 + 15) < 64, "wo arrives too late");
    lds_wait<1>(&sh->flag[4]);
    const f2 g = sh->gml;
    float fw = __expf(mw - g[0]) * g[1];
    PIN(fw);
    constexpr int PD = pv_done(63);
#pragma unroll
    for (int j = PD; j < 32; ++j) { PV(j); SB(); }
    STAMP(7);
    float t4[4];
#pragma unroll
    for (int i = 0; i < 4; ++i) t4[i] = sw32(acc[i], acc[i + 4]);
    sh->ob[wave][4 * (lane & 15) + (lane >> 4)] = pk(sw16(t4[0], t4[2]) * fw, sw16(t4[1], t4[3]) * fw);
    tail<true>(c, sh, rws, WF, gw
               , ts
    );
  } else {
    const u32 ep = __builtin_amdgcn_raw_buffer_load_b32(rws, O_EPOCH, 0, SC1);
    const int wo_vo = wo_row + 1024 + 32 * (wave - 8) + 4 * (lane & 7);
    u1 wr[16];
    u4 WF[4], gw[8];
#define LD_WO() do { _Pragma("unroll") for (int i = 0; i < 16; ++i) wr[i][0] = __builtin_amdgcn_raw_buffer_load_b32(rwo, wo_vo, (64 * (i >> 3) + (i & 7)) * (D * 2), NT); SB(); } while (0)
    if (wave < 11) {
      const int qt = b < 72 ? b / 18 : (b - 72) / 36, ln = b < 72 ? b - 18 * qt : b - 54 - 36 * qt;
      const int j = wave - 8, r0 = 144 * qt + 48 * j + 6 * (lane >> 3);
      const rsrc_t rc = mkrs(cv, D * 2), rm = mkrs(wmod, b < 216 ? D * 6 * D * 2 : 0);
      const int mo = r0 * (6 * D * 2) + (64 * ln + 8 * (lane & 7)) * 2;
      u4 wm[6];
      const u3 cr = __builtin_amdgcn_raw_buffer_load_b96(rc, r0 * 2, 0, 0);
#pragma unroll
      for (int i = 0; i < 6; ++i) wm[i] = __builtin_amdgcn_raw_buffer_load_b128(rm, mo, i * (6 * D * 2), NT);
      SB();
      bar();
      LD_WO();
      float a[8];
#pragma unroll
      for (int e = 0; e < 8; ++e) a[e] = 0.f;
      u32 act[3];
#pragma unroll
      for (int p = 0; p < 3; ++p) { const u32 w = cr[p]; act[p] = pk(silu(lo16(w)), silu(hi16(w))); PIN(act[p]); }
      SB();
#pragma unroll
      for (int p = 0; p < 3; ++p)
#pragma unroll
        for (int e = 0; e < 4; ++e) { const u32 u = wm[2 * p][e], v = wm[2 * p + 1][e]; a[2 * e] = dot2(plo(u, v), act[p], a[2 * e]); a[2 * e + 1] = dot2(phi(u, v), act[p], a[2 * e + 1]); }
      float t[4], u[2];
#pragma unroll
      for (int i = 0; i < 4; ++i) t[i] = sw32(a[i], a[i + 4]);
#pragma unroll
      for (int i = 0; i < 2; ++i) u[i] = sw16(t[i], t[i + 2]);
      const bool b3 = lane & 8;
      sh->mp[j][8 * (lane & 7) + 2 * (lane >> 4) + ((lane >> 3) & 1)] = (b3 ? u[1] : u[0]) + dpp<ROR8>(b3 ? u[0] : u[1]);
      lds_set(&sh->flag[j], lane);
      STAMP(1);
      if (wave == 8 && b < 216) {
        lds_wait<1>(&sh->flag[1]); lds_wait<1>(&sh->flag[2]);
        const int l = 2 * (lane & 31);
        const f2 m = *(const LAS f2*)&sh->mp[0][l] + *(const LAS f2*)&sh->mp[1][l] + *(const LAS f2*)&sh->mp[2][l];
        const float m0 = m[0], m1 = m[1];
        const u4 o = {asu(m0), ep + 1, asu(m1), ep + 1};
        if (lane < 32) st128(o, rws, O_MOD + qt * MOD_STR + ln * 512 + lane * 16);
        STAMP(2);
      }
    } else {
      const rsrc_t rx = mkrs(x, D * 2), rq = mkrs(wq, D * 4096 * 2);
      const int g = lane >> 4, pc = (lane & 15) < 9 ? (lane & 15) : 8, pidx = 36 * s + 9 * g + pc;
      const u4 xa = __builtin_amdgcn_raw_buffer_load_b128(rx, lane * 16, 0, 0);
      const u4 xb = __builtin_amdgcn_raw_buffer_load_b128(rx, 1024 + lane * 16, 0, 0);
      const u32 xp = __builtin_amdgcn_raw_buffer_load_b32(rx, pidx * 4, 0, 0);
      if (lane < 16) { sh->flag[lane] = 0; f2 z = {0.f, 0.f}; sh->st[lane] = z; }
      { f2 z = {0.f, 0.f}; *(LAS f2*)&sh->gp[lane][18] = z; }
      SB();
      bar();
      u4 wv[18];
      const int qo = (72 * s + 18 * g) * 8192 + (128 * h + 8 * (lane & 15)) * 2;
#pragma unroll
      for (int i = 0; i < 18; ++i) wv[i] = __builtin_amdgcn_raw_buffer_load_b128(rq, qo, i * 8192, NT);
      SB();
      LD_WO();
      const u32 tag = ep + 1;
      sh->tag = tag;
      float l0, l1;
      {
        float sum = 0.f;
#pragma unroll
        for (int i = 0; i < 4; ++i) { const u32 u = xa[i], v = xb[i]; sum += (lo16(u) + hi16(u)) + (lo16(v) + hi16(v)); }
        const float mean = wave_sum(sum) * (1.f / D);
        float sa = 0.f, sb = 0.f;
#pragma unroll
        for (int i = 0; i < 4; ++i) {
          const u32 u = xa[i], v = xb[i];
          const float d0 = lo16(u) - mean, d1 = hi16(u) - mean, d2 = lo16(v) - mean, d3 = hi16(v) - mean;
          sa = fmaf(d0, d0, fmaf(d1, d1, sa)); sb = fmaf(d2, d2, fmaf(d3, d3, sb));
        }
        const float rs = ln_rstd(wave_sum(lane < 8 ? sa + sb : sa) * (1.f / D));
        l0 = rb(rb(lo16(xp) - rb(mean)) * rs); l1 = rb(rb(hi16(xp) - rb(mean)) * rs);
        PIN(l0); PIN(l1);
      }
      u32 P[9][8];
#pragma unroll
      for (int p = 0; p < 9; ++p)
#pragma unroll
        for (int e = 0; e < 4; ++e) { const u32 u = wv[2 * p][e], v = wv[2 * p + 1][e]; P[p][2 * e] = plo(u, v); P[p][2 * e + 1] = phi(u, v); PIN(P[p][2 * e]); PIN(P[p][2 * e + 1]); }
      STAMP(1);
      {
        int o = O_MOD + pidx * 16;
        u4 v[8];
#pragma nounroll
        for (u32 n = 0; n < SPIN_G; ++n) {
          FENCE(); PIN(o);
          u32 bad = 0;
#pragma unroll
          for (int i = 0; i < 8; ++i) v[i] = ld128(rws, o + (i >> 1) * MOD_STR + (i & 1) * 4608);
#pragma unroll
          for (int i = 0; i < 8; ++i) bad |= (v[i][1] ^ tag) | (v[i][3] ^ tag);
          if (__all(bad == 0)) break;
        }
        STAMP(2);
        float m[4];
#pragma unroll
        for (int i = 0; i < 4; ++i) {
          const u32 e0 = v[i >> 1][2 * (i & 1)], e1 = v[2 + (i >> 1)][2 * (i & 1)], e2 = v[4 + (i >> 1)][2 * (i & 1)], e3 = v[6 + (i >> 1)][2 * (i & 1)];
          m[i] = rb((asf(e0) + asf(e1)) + (asf(e2) + asf(e3)));
        }
        const u32 hp = pk(rb(l0 * rb(1.f + m[2])) + m[0], rb(l1 * rb(1.f + m[3])) + m[1]);
        float a[8];
#pragma unroll
        for (int e = 0; e < 8; ++e) a[e] = 0.f;
#pragma unroll
        for (int p = 0; p < 9; ++p) {
          const u32 hb = nbc(hp, p);
#pragma unroll
          for (int e = 0; e < 8; ++e) a[e] = dot2(P[p][e], hb, a[e]);
        }
        float t[4];
#pragma unroll
        for (int i = 0; i < 4; ++i) t[i] = sw32(a[i], a[i + 4]);
        const u4 qo4 = {asu(sw16(t[0], t[2])), tag, asu(sw16(t[1], t[3])), tag};
        st128(qo4, rws, O_QP + c.pi * 1024 + lane * 16);
      }
      {
        int o = O_QP + h * 8192 + lane * 16;
        u4 v[8];
#pragma nounroll
        for (u32 n = 0; n < SPIN_G; ++n) {
          FENCE(); PIN(o);
          u32 bad = 0;
#pragma unroll
          for (int i = 0; i < 8; ++i) v[i] = ld128(rws, o + i * 1024);
#pragma unroll
          for (int i = 0; i < 8; ++i) bad |= (v[i][1] ^ tag) | (v[i][3] ^ tag);
          if (__all(bad == 0)) break;
        }
        float q0 = 0.f, q1 = 0.f;
#pragma unroll
        for (int i = 0; i < 8; ++i) { const u32 u = v[i][0], w = v[i][2]; q0 += asf(u); q1 += asf(w); }
        sh->q[4 * (lane & 15) + g] = pk(q0, q1);
        lds_set(&sh->flag[3], lane);
      }
      STAMP(3);
      const bool red = b >= 64 && b < 100;
      if (red) {
        const int pr = 8 * (b - 64) + (lane & 7), vec = (lane >> 3) & 3;
        const u32 xq = __builtin_amdgcn_raw_buffer_load_b32(rx, pr * 4, 0, 0);
        int o = O_MOD + (lane >> 5) * 2 * MOD_STR + (2 + vec) * 4608 + pr * 16;
        u4 v0, v1;
#pragma nounroll
        for (u32 n = 0; n < SPIN_G; ++n) {
          FENCE(); PIN(o);
          v0 = ld128(rws, o); v1 = ld128(rws, o + MOD_STR);
          if (__all(((v0[1] ^ tag) | (v0[3] ^ tag) | (v1[1] ^ tag) | (v1[3] ^ tag)) == 0)) break;
        }
        const u32 a0 = v0[0], a1 = v0[2], b0 = v1[0], b1 = v1[2];
        const float m0 = asf(a0) + asf(b0), m1 = asf(a1) + asf(b1);
        const u32 d = pk(sw32(m0, m0), sw32(m1, m1));
        const u4 o4 = {d, tag, dppu<SHL + 1>(d), tag};
        if (lane >= 8 && lane < 24 && !(lane & 1)) st128(o4, rws, O_SC + ((vec - 1) * 288 + pr) * 8);
        if (lane < 32) sh->sc[lane] = vec == 1 ? xq : d;
      }
      {
#pragma nounroll
        for (u32 n = 0; n < SPIN_L; ++n) {
          if (__all(((volatile LAS u32*)sh->flag)[8 + (lane & 7)] != 0)) break;
          __builtin_amdgcn_s_sleep(4);
        }
        FENCE();
        const f2 m = sh->ml[lane & 7];
        const float M = max8(m[0]);
        const float L = sum8(m[1] * __expf(m[0] - M));
        const u4 o = {asu(M), tag, asu(L), tag};
        if (lane == 0) st128(o, rws, O_ML + c.pi * 16);
        const f2 w = {M, 1.f / L};
        sh->gml = w;
        lds_set(&sh->flag[4], lane);
      }
      STAMP(4);
      if (ffn) {
        int o = O_SC + lane * 16;
        u4 v[5];
#pragma nounroll
        for (u32 n = 0; n < SPIN_G; ++n) {
          FENCE(); PIN(o);
          u32 bad = 0;
#pragma unroll
          for (int i = 0; i < 5; ++i) v[i] = ld128(rws, o + (i < 4 ? i * 1024 : 3584));
#pragma unroll
          for (int i = 0; i < 5; ++i) bad |= (v[i][1] ^ tag) | (v[i][3] ^ tag);
          if (__all(bad == 0)) break;
          __builtin_amdgcn_s_sleep(16);
        }
#pragma unroll
        for (int i = 0; i < 5; ++i) { const u2 w = {v[i][0], v[i][2]}; *(LAS u2*)&sh->sc[(i < 4 ? 128 * i : 448) + 2 * lane] = w; }
      } else if (red) {
        int o = O_ML + lane * 16;
        u4 v[4];
#pragma nounroll
        for (u32 n = 0; n < SPIN_G; ++n) {
          FENCE(); PIN(o);
          u32 bad = 0;
#pragma unroll
          for (int i = 0; i < 4; ++i) v[i] = ld128(rws, o + i * 1024);
#pragma unroll
          for (int i = 0; i < 4; ++i) bad |= (v[i][1] ^ tag) | (v[i][3] ^ tag);
          if (__all(bad == 0)) break;
          __builtin_amdgcn_s_sleep(8);
        }
#pragma unroll
        for (int i = 0; i < 4; ++i) {
          const u32 mu = v[i][0], lu = v[i][2];
          const float e = asf(lu) * __expf(asf(mu) - max8(asf(mu)));
          sh->wt[64 * i + lane] = e / sum8(e);
        }
      }
      STAMP(6);
    }
    frag<1>(wr, WF);
    frag<1>(wr + 8, WF + 2);
    SB();
    if (wave < 11) lds_wait<8>(&sh->flag[4]);
    STAMP(7);
    tail<false>(c, sh, rws, WF, gw
                , ts
    );
  }
}

__global__ void k1(u32* p) { *p = (u32)__builtin_amdgcn_s_memrealtime(); }
extern "C" void kernel_launch(void* const* d_in, const int* in_sizes, int n_in, void* d_out, int out_size, void* d_ws, size_t ws_size,
                              hipStream_t stream) {
k1<<<1, 64, 0, stream>>>((u32*)((char*)d_ws + (8 << 20)));
  fused_kernel<<<256, 768, 0, stream>>>((const u16*)d_in[0], (const u16*)d_in[1], (const u16*)d_in[2], (const u16*)d_in[3],
                                        (const u16*)d_in[4], (const u16*)d_in[6], (const u16*)d_in[7], (const u16*)d_in[8],
                                        (const u16*)d_in[9], (const u16*)d_in[10], (u16*)d_out, (char*)d_ws);

}
```

```cpp
#include <hip/hip_runtime.h>
#include <stdint.h>

#define STAMPS 0
#define MAPB 0
#define PS 0
#define P0 8
#define POFF 0
#define PKNOTS 125, 225, 400, 670, 960, 1210, 1490, 1730, 1955, 2180
#define GUNT 1
#define KVA 2
#define PLA 16
#define STA 16
#define SMA 0
#define X_NOWO 0
#define X_NOGU 0
#define X_NOPOLL 0
#define PRIO 0
#define X_CONT 0
#define WSE 0
#define WSO(n) (WSE ? ((n) - (size_t)WSE * 1048576) & ~(size_t)0xfffff : 0)
#define WK 3
#define WV 4
#define T4 64
#define PV0 4
#define GN 1
#define WDE 0
#define WQW 18
#define LATE 54
#define CPF 21500
#define PSL 16
#define TP1 700
#define TP2 1000
#define HW 1
#define DRY 0

typedef unsigned u32;
typedef unsigned long long u64;
typedef unsigned short u16;
typedef u32 u1 __attribute__((ext_vector_type(1)));
typedef u32 u2 __attribute__((ext_vector_type(2)));
typedef u32 u3 __attribute__((ext_vector_type(3)));
typedef u32 u4 __attribute__((ext_vector_type(4)));
typedef float f2 __attribute__((ext_vector_type(2)));
typedef float f4 __attribute__((ext_vector_type(4)));
typedef __bf16 bf2 __attribute__((ext_vector_type(2)));
typedef __bf16 bf8 __attribute__((ext_vector_type(8)));
typedef __amdgpu_buffer_rsrc_t rsrc_t;
#define LAS __attribute__((address_space(3)))
#define GAS __attribute__((address_space(1)))
#define DI __device__ __forceinline__

constexpr int D = 576, FF = 4608;
constexpr int NT = KVA;
#define PLS " sc1"
constexpr u32 SPIN_G = 1u << 17, SPIN_L = 1u << 20;

constexpr int O_EPOCH = 0;
constexpr int O_SC = 4096;
constexpr int O_ML = 12288;
constexpr int O_MOD = 16384, MOD_STR = 3456 * 8;
constexpr int O_X2 = 131072;
constexpr int O_QP = 262144;
constexpr int O_OP = O_QP + 262144;
constexpr int O_YA = O_OP + 131072;
constexpr int O_FC = O_YA + 36 * 32 * 128;
constexpr int O_END = O_FC + 36 * 144 * 128;
constexpr int O_STAMP = 4 << 20;
constexpr int RED0 = 76;

DI float asf(u32 x) { return __uint_as_float(x); }
DI u32 asu(float x) { return __float_as_uint(x); }
DI u32 pk(float lo, float hi) { f2 t = {lo, hi}; bf2 r = __builtin_convertvector(t, bf2); return __builtin_bit_cast(u32, r); }
DI float rb(float x) { return asf(pk(0.f, x)); }
DI float lo16(u32 x) { return asf(x << 16); }
DI float hi16(u32 x) { return asf(x & 0xffff0000u); }
template <int C> DI u32 dppu(u32 x) { return (u32)__builtin_amdgcn_update_dpp(0, (int)x, C, 0xf, 0xf, true); }
template <int C> DI float dpp(float x) { return asf(dppu<C>(asu(x))); }
constexpr int ROR4 = 0x124, ROR8 = 0x128, HMIR = 0x141, QX1 = 0xB1, QX2 = 0x4E, SHL = 0x100, NBC = 0x150;
DI u32 nbc(u32 src, int n) {
  switch (n) {
#define CASE(n) case n: return dppu<NBC + n>(src);
    CASE(0) CASE(1) CASE(2) CASE(3) CASE(4) CASE(5) CASE(6) CASE(7) CASE(8) CASE(9) CASE(10) CASE(11) CASE(12) CASE(13) CASE(14)
#undef CASE
    default: return dppu<NBC + 15>(src);
  }
}
DI float dot2(u32 a, u32 b, float c) { return __builtin_amdgcn_fdot2_f32_bf16(__builtin_bit_cast(bf2, a), __builtin_bit_cast(bf2, b), c, false); }
DI float sw32(float a, float b) { auto r = __builtin_amdgcn_permlane32_swap(asu(a), asu(b), false, false); u32 r0 = r[0], r1 = r[1]; return asf(r0) + asf(r1); }
DI float sw16(float a, float b) { auto r = __builtin_amdgcn_permlane16_swap(asu(a), asu(b), false, false); u32 r0 = r[0], r1 = r[1]; return asf(r0) + asf(r1); }
DI float mx32(float a) { auto r = __builtin_amdgcn_permlane32_swap(asu(a), asu(a), false, false); u32 r0 = r[0], r1 = r[1]; return fmaxf(asf(r0), asf(r1)); }
DI float mx16(float a) { auto r = __builtin_amdgcn_permlane16_swap(asu(a), asu(a), false, false); u32 r0 = r[0], r1 = r[1]; return fmaxf(asf(r0), asf(r1)); }
DI float row_sum(float v) { v += dpp<QX1>(v); v += dpp<QX2>(v); v += dpp<ROR4>(v); return v + dpp<ROR8>(v); }
DI float wave_sum(float v) { v = row_sum(v); v = sw16(v, v); return sw32(v, v); }
DI float wave_max(float v) {
  v = fmaxf(v, dpp<QX1>(v)); v = fmaxf(v, dpp<QX2>(v)); v = fmaxf(v, dpp<ROR4>(v)); v = fmaxf(v, dpp<ROR8>(v));
  return mx32(mx16(v));
}
DI float sum8(float v) { v += dpp<QX1>(v); v += dpp<QX2>(v); return v + dpp<HMIR>(v); }
DI float max8(float v) { v = fmaxf(v, dpp<QX1>(v)); v = fmaxf(v, dpp<QX2>(v)); return fmaxf(v, dpp<HMIR>(v)); }
DI u32 plo(u32 a, u32 b) { return __builtin_amdgcn_perm(b, a, 0x05040100u); }
DI u32 phi(u32 a, u32 b) { return __builtin_amdgcn_perm(b, a, 0x07060302u); }
DI f4 mfma(u4 a, u4 b, f4 c) { return __builtin_amdgcn_mfma_f32_16x16x32_bf16(__builtin_bit_cast(bf8, a), __builtin_bit_cast(bf8, b), c, 0, 0, 0); }
DI rsrc_t mkrs(const void* p, u32 n) { return __builtin_amdgcn_make_buffer_rsrc((void*)p, 0, n, 0x00020000); }
#define FENCE() asm volatile("" ::: "memory")
#define SB() __builtin_amdgcn_sched_barrier(0)
#define PIN(v) asm volatile("" : "+v"(v))
#define VMCNT(n) asm volatile("s_waitcnt vmcnt(%0)" ::"n"(n) : "memory")
#define LDSW() asm volatile("s_waitcnt lgkmcnt(0)" ::: "memory")
DI void bar() { asm volatile("s_waitcnt lgkmcnt(0)\n\ts_barrier" ::: "memory"); }
DI float silu(float v) { return v * __builtin_amdgcn_rcpf(1.f + __expf(-v)); }
DI u4 ld128(rsrc_t r, int o) { return __builtin_amdgcn_raw_buffer_load_b128(r, o, 0, PLA); }
DI u2 ld64(rsrc_t r, int o) { return __builtin_amdgcn_raw_buffer_load_b64(r, o, 0, PLA); }
DI void st128(u4 v, rsrc_t r, int o) { __builtin_amdgcn_raw_buffer_store_b128(v, r, o, 0, STA); }
DI float ln_rstd(float var) { return __builtin_amdgcn_rcpf(rb(__builtin_amdgcn_sqrtf(rb(rb(var) + 1e-5f)))); }

struct __attribute__((aligned(16))) Sh {
  float gp[64][20];
  f2 ob[8][72];
  float mp[3][64];
  u32 sc[576];
  u32 q[64];
  f2 st[16];
  f2 ml[8];
  float gm;
  u32 tag;
  u32 pad[2];
  u32 flag[16];
};

#define STAMP(i)
#define STAMP_OUT()

#define STAMPT(i) STAMP(i)
#define STAMPK(i)
template <int S> DI void lds_wait(volatile LAS u32* f) {
#pragma nounroll
  for (u32 n = 0; n < SPIN_L; ++n) { if (*f != 0) break; __builtin_amdgcn_s_sleep(S); }
  FENCE();
}
DI void until(u32 tz, u32 t) {
#pragma nounroll
  for (u32 n = 0; n < 4096; ++n) { if ((u32)__builtin_amdgcn_s_memrealtime() - tz >= t) break; __builtin_amdgcn_s_sleep(2); }
}
DI void lds_set(LAS u32* f, int lane) { LDSW(); if (lane == 0) *(volatile LAS u32*)f = 1; }

DI float tred16(const float* s, int lane) {
  float t[8], u[4], v[2];
  const bool b3 = lane & 8, b2 = lane & 4, b1 = lane & 2, b0 = lane & 1;
#pragma unroll
  for (int i = 0; i < 8; ++i) t[i] = (b3 ? s[i + 8] : s[i]) + dpp<ROR8>(b3 ? s[i] : s[i + 8]);
#pragma unroll
  for (int i = 0; i < 4; ++i) u[i] = (b2 ? t[i + 4] : t[i]) + dpp<HMIR>(b2 ? t[i] : t[i + 4]);
#pragma unroll
  for (int i = 0; i < 2; ++i) v[i] = (b1 ? u[i + 2] : u[i]) + dpp<QX2>(b1 ? u[i] : u[i + 2]);
  return (b0 ? v[1] : v[0]) + dpp<QX1>(b0 ? v[0] : v[1]);
}

template <int C, typename V> DI void frag(const V* L, u4* F) {
#pragma unroll
  for (int c = 0; c < C; ++c) {
    u4 a, b;
#pragma unroll
    for (int j = 0; j < 4; ++j) { const u32 x = L[2 * j][c], y = L[2 * j + 1][c]; a[j] = plo(x, y); b[j] = phi(x, y); }
    PIN(a); PIN(b);
    F[2 * c] = a; F[2 * c + 1] = b;
  }
}

DI u4 gather8(float v) {
  const u32 d = pk(v, dpp<SHL + 1>(v));
  u4 a; a[0] = d; a[1] = dppu<SHL + 2>(d); a[2] = dppu<SHL + 4>(d); a[3] = dppu<SHL + 6>(d);
  return a;
}

template <int I> struct IC { static constexpr int value = I; };
template <int I, int N, class F> DI void sfor(F&& f) { if constexpr (I < N) { f(IC<I>{}); sfor<I + 1, N>(f); } }

DI void swap16(u32 d, u32& r0, u32& r1) { auto r = __builtin_amdgcn_permlane16_swap(d, d, false, false); r0 = r[0]; r1 = r[1]; }
#define LD_GU(j) __builtin_nontemporal_load((const GAS u4*)(gup + (j) * gstr))

template <int N, int STR, int SL> DI void poll(u4* v, u4 rs, int off, u32 tag, int pass) {
  u64 m[N], any;
#pragma unroll
  for (int i = 0; i < N; ++i) { m[i] = ~0ull; asm volatile("" : "=v"(v[i])); }
  int n = pass * SPIN_G;
#pragma nounroll
  do {
#pragma unroll
    for (int i = 0; i < N; ++i)
      asm volatile("s_mov_b64 exec, %2\n\tbuffer_load_dwordx4 %0, %1, %3, %4 offen" PLS "\n\ts_mov_b64 exec, -1" : "+v"(v[i]) : "v"(off), "s"(m[i]), "s"(rs), "s"(i * STR) : "memory");
    VMCNT(0);
    any = 0;
#pragma unroll
    for (int i = 0; i < N; ++i) { PIN(v[i]); const u32 t1 = v[i][1], t3 = v[i][3]; m[i] = __ballot(t1 != tag) | __ballot(t3 != tag); any |= m[i]; }
    if (--n < 0) any = 0;
    if (SL && any) __builtin_amdgcn_s_sleep(SL);
  } while (any);
}
template <int N> DI f4 rec_sum(const u4* v, u4 sel) {
  const f4 z = {0.f, 0.f, 0.f, 0.f};
  f4 d0 = mfma(sel, v[0], z), d1 = mfma(sel, v[1], z);
#pragma unroll
  for (int i = 2; i < N; ++i) { if (i & 1) d1 = mfma(sel, v[i], d1); else d0 = mfma(sel, v[i], d0); }
  d0 += d1;
  f4 r;
#pragma unroll
  for (int i = 0; i < 4; ++i) { float t = d0[i]; t += dpp<ROR4>(t); r[i] = t + dpp<ROR8>(t); }
  return r;
}

constexpr int win(int t) { return t >= T4 ? WV : WK; }
constexpr int due(int t) {
  constexpr int kt[] = {3, 6, 10, 16, 24, 32, 40, 48, 56, 64}, kv[] = {PKNOTS};
  int i = 0;
  while (kt[i + 1] <= t) ++i;
  return POFF + (kv[i] + (kv[i + 1] - kv[i]) * (t - kt[i]) / (kt[i + 1] - kt[i])) * PS / 1000;
}
constexpr int QS = 32 + PV0 - 1;
constexpr int pv_done(int t) {
  int n = 0;
  for (int i = QS + 5; i <= t; ++i) { const int av = i - 32 - win(i) + 1; n = n + 3 < av ? n + 3 : (av > n ? av : n); }
  return n;
}

__global__ void __launch_bounds__(768, 3)
fused_kernel(const u16* x, const u16* kc, const u16* vc, const u16* wq, const u16* wo, const u16* wg, const u16* wu, const u16* wd,
             const u16* cv, const u16* wmod, u16* out, char* ws) {
  __shared__ Sh shm;
  LAS Sh* sh = (LAS Sh*)&shm;
  STAMP(0);
  const int lane = threadIdx.x & 63, wave = __builtin_amdgcn_readfirstlane(threadIdx.x >> 6);
  const int b = blockIdx.x, xc = b & 7, ci = b >> 3, s = MAPB ? xc : ci & 7, h = MAPB ? ci : (ci >> 3) * 8 + xc, pi = 8 * h + s;
  u32 cpf = 0;
  if (CPF) cpf = __builtin_amdgcn_raw_buffer_load_b32(mkrs((const void*)(__builtin_amdgcn_s_getpc() & ~255ull), CPF), (((ci & 3) * 64 + lane) * 128) | (wave == 11 ? 0 : 1 << 30), 0, SMA);
  const u32 tz = (u32)__builtin_amdgcn_s_memrealtime();
  SB();
  const size_t slab = ((size_t)h * 8192 + s * 1024) * 128;
  const int vo = (wave >> 2) * 131072 + (wave & 3) * 1024 + lane * 16;
  asm volatile("" : "+s"(x), "+s"(kc), "+s"(vc), "+s"(wq), "+s"(wo), "+s"(wg), "+s"(wu), "+s"(wd), "+s"(cv), "+s"(wmod), "+s"(out), "+s"(ws));
  const rsrc_t rk = mkrs(kc + slab, 262144);
  u4 kb[32];
#pragma unroll
  for (int t = 0; t < WK; ++t) kb[t] = __builtin_amdgcn_raw_buffer_load_b128(rk, vo, t * 4096, NT);
  SB();
  if (threadIdx.x < 16) sh->flag[threadIdx.x] = 0;
  bar();
  const bool ffn = b >= 112;
  const int unit = wave < 8 ? wave + 4 : wave - 8;
  u4 F[20], G0, G1, G2, G3;
  u32 tag;
  float cw = 0.f;
#define GUP() \
    const bool gu_ = ffn && unit < 9; \
    const u32 gstr = gu_ ? ((X_CONT & 2) ? 512 * 9 : (X_CONT & 4) ? FF * 2 : FF * 2) : 0; \
    const char* gup = (X_CONT & 2) && gu_ ? (const char*)((lane & 32) ? wu : wg) + (b - 112) * 36864 + unit * 512 + (lane & 31) * 16 : (X_CONT & 4) && gu_ ? (const char*)(((ci & 1) ? wu : wg) + (size_t)(64 * unit + 8 * (lane >> 3)) * FF + 576 * xc + 32 * ((ci & ~1) - 14) + 8 * (lane & 7)) : gu_ ? (const char*)(((lane & 8) ? wu : wg) + (size_t)(64 * unit + 16 * (lane >> 4) + 2 * (lane & 4)) * FF + 576 * xc + 32 * (ci - 14) + 8 * (lane & 3)) : (const char*)wg; \
    PIN(gup)
#define LD_WD(n, W) do { \
      const rsrc_t rwd = mkrs(wd + (size_t)(576 * xc + 32 * (ci - 14)) * D, n); \
      const int o_ = 8 * (lane >> 4) * (D * 2) + (192 * (wave - 9) + 4 * (lane & 15)) * 2; \
      u2 w_[3][8]; \
      _Pragma("unroll") for (int j = 0; j < 8; ++j) { \
        if (W && j >= W) VMCNT(3 * W - 3); \
        _Pragma("unroll") for (int e = 0; e < 3; ++e) w_[e][j] = __builtin_amdgcn_raw_buffer_load_b64(rwd, o_ + e * 128, j * (D * 2), NT); \
        SB(); } \
      _Pragma("unroll") for (int e = 0; e < 3; ++e) frag<2>(w_[e], F + 8 + 4 * e); \
      SB(); } while (0)

  if (__builtin_expect(wave >= 8, 1)) {
    STAMPK(10);
    const rsrc_t rws = mkrs(ws, O_END);
    if (__builtin_expect(wave < 11, 1)) {
      const int qt = b < 72 ? (b * 57) >> 10 : ((b - 72) * 57) >> 11, ln = b < 72 ? b - 18 * qt : b - 54 - 36 * qt;
      const int j = wave - 8, r0 = 144 * qt + 48 * j + 6 * (lane >> 3);
      const rsrc_t rc = mkrs(cv, D * 2), rm = mkrs(wmod, b < 216 ? D * 6 * D * 2 : 0);
      const int mo = r0 * (6 * D * 2) + (64 * ln + 8 * (lane & 7)) * 2;
      u4 wm[6];
#pragma unroll
      for (int i = 0; i < 6; ++i) wm[i] = __builtin_amdgcn_raw_buffer_load_b128(rm, mo, i * (6 * D * 2), NT);
      const u3 cr = __builtin_amdgcn_raw_buffer_load_b96(rc, r0 * 2, 0, SMA);
      const u32 ep = __builtin_amdgcn_raw_buffer_load_b32(rws, O_EPOCH, 0, PLA);
      SB();
      STAMPK(11);
      float a[8];
#pragma unroll
      for (int e = 0; e < 8; ++e) a[e] = 0.f;
      u32 P[3][8];
#pragma unroll
      for (int p = 0; p < 3; ++p)
#pragma unroll
        for (int e = 0; e < 4; ++e) { const u32 u = wm[2 * p][e], v = wm[2 * p + 1][e]; P[p][2 * e] = plo(u, v); P[p][2 * e + 1] = phi(u, v); }
#pragma unroll
      for (int p = 0; p < 3; ++p) {
        const u32 w = cr[p], act = pk(silu(lo16(w)), silu(hi16(w)));
#pragma unroll
        for (int e = 0; e < 8; ++e) a[e] = dot2(P[p][e], act, a[e]);
      }
      float t[4], u[2];
#pragma unroll
      for (int i = 0; i < 4; ++i) t[i] = sw32(a[i], a[i + 4]);
#pragma unroll
      for (int i = 0; i < 2; ++i) u[i] = sw16(t[i], t[i + 2]);
      const bool b3 = lane & 8;
      sh->mp[j][8 * (lane & 7) + 2 * (lane >> 4) + ((lane >> 3) & 1)] = (b3 ? u[1] : u[0]) + dpp<ROR8>(b3 ? u[0] : u[1]);
      lds_set(&sh->flag[j], lane);
      tag = __builtin_amdgcn_readfirstlane(((ep | 0x40004000u) + 1) & 0xbfffbfffu);
      STAMP(1);
      if (wave == 8 && b < 216) {
        lds_wait<1>(&sh->flag[1]); lds_wait<1>(&sh->flag[2]);
        const int l = 2 * (lane & 31);
        const f2 m = *(const LAS f2*)&sh->mp[0][l] + *(const LAS f2*)&sh->mp[1][l] + *(const LAS f2*)&sh->mp[2][l];
        const float m0 = m[0], m1 = m[1];
        const u4 o = {asu(m0), tag, asu(m1), tag};
        if (lane < 32) st128(o, rws, O_MOD + qt * MOD_STR + ln * 512 + lane * 16);
        STAMP(2);
      }
      if (b < 96) {
        const rsrc_t rwo = mkrs(wo + (size_t)(b & 31) * 128 * D, X_NOWO ? 0 : 128 * D * 2);
        const int o = (X_CONT & 1) ? (b >> 5) * 49152 + j * 1024 + lane * 16 : (16 * (lane >> 4) + (lane & 8)) * (D * 2) + 128 * (3 * (b >> 5) + j) + 16 * (lane & 7);
        u4 w[16];
#pragma unroll
        for (int i = 0; i < 16; ++i) { if (i >= 2 * HW) VMCNT(2 * HW - 1); w[i] = __builtin_amdgcn_raw_buffer_load_b128(rwo, o, (X_CONT & 1) ? i * 3072 : (64 * (i >> 3) + (i & 7)) * (D * 2), NT); SB(); }
        frag<4>(w, F);
        frag<4>(w + 8, F + 8);
      } else if (ffn && !X_NOGU) {
        GUP();
        u4 G[8];
        sfor<0, 8>([&](auto I) __attribute__((always_inline)) { if (I.value >= HW) VMCNT(HW - 1); G[I.value] = LD_GU(I.value); SB(); });
        frag<4>(G, F);
        if (WDE && wave > 8) LD_WD(32 * D * 2, 2);
      }
      STAMP(5);
    } else {
      const rsrc_t rx = mkrs(x, D * 2), rq = mkrs(wq, D * 4096 * 2);
      const int g = lane >> 4, pc = (lane & 15) < 9 ? (lane & 15) : 8, pidx = 36 * s + 9 * g + pc;
      const u4 xa = __builtin_amdgcn_raw_buffer_load_b128(rx, lane * 16, 0, SMA);
      const u4 xb = __builtin_amdgcn_raw_buffer_load_b128(rx, 1024 + lane * 16, 0, SMA);
      const u32 xp = __builtin_amdgcn_raw_buffer_load_b32(rx, pidx * 4, 0, SMA);
      const u32 ep = __builtin_amdgcn_raw_buffer_load_b32(rws, O_EPOCH, 0, PLA);
      u4 wv[18];
      const int qo = (72 * s + 18 * g) * 8192 + (128 * h + 8 * (lane & 15)) * 2;
#pragma unroll
      for (int i = 0; i < 18; ++i) { if (i >= WQW) VMCNT(WQW - 1); wv[i] = __builtin_amdgcn_raw_buffer_load_b128(rq, qo, i * 8192, NT); SB(); }
      STAMPK(11);
      { f2 z = {0.f, 0.f}; *(LAS f2*)&sh->gp[lane][18] = z; sh->st[lane & 15] = z; }
      tag = __builtin_amdgcn_readfirstlane(((ep | 0x40004000u) + 1) & 0xbfffbfffu);
      sh->tag = tag;
      float l0, l1;
      {
        float sum = 0.f;
#pragma unroll
        for (int i = 0; i < 4; ++i) { const u32 u = xa[i], v = xb[i]; sum += (lo16(u) + hi16(u)) + (lo16(v) + hi16(v)); }
        const float mean = wave_sum(sum) * (1.f / D);
        float sa = 0.f, sb = 0.f;
#pragma unroll
        for (int i = 0; i < 4; ++i) {
          const u32 u = xa[i], v = xb[i];
          const float d0 = lo16(u) - mean, d1 = hi16(u) - mean, d2 = lo16(v) - mean, d3 = hi16(v) - mean;
          sa = fmaf(d0, d0, fmaf(d1, d1, sa)); sb = fmaf(d2, d2, fmaf(d3, d3, sb));
        }
        const float rs = ln_rstd(wave_sum(lane < 8 ? sa + sb : sa) * (1.f / D));
        l0 = rb(rb(lo16(xp) - rb(mean)) * rs); l1 = rb(rb(hi16(xp) - rb(mean)) * rs);
        PIN(l0); PIN(l1);
      }
      u32 P[9][8];
#pragma unroll
      for (int p = 0; p < 9; ++p)
#pragma unroll
        for (int e = 0; e < 4; ++e) { const u32 u = wv[2 * p][e], v = wv[2 * p + 1][e]; P[p][2 * e] = plo(u, v); P[p][2 * e + 1] = phi(u, v); PIN(P[p][2 * e]); PIN(P[p][2 * e + 1]); }
      STAMP(1);
      {
        int o = (X_NOPOLL & 1) ? O_END : O_MOD + pidx * 16;
        u4 v[8];
        if (PRIO) __builtin_amdgcn_s_setprio(PRIO);
        until(tz, TP1);
#pragma nounroll
        for (u32 n = 0; n < SPIN_G; ++n) {
          FENCE(); PIN(o);
          u32 bad = 0;
#pragma unroll
          for (int i = 0; i < 8; ++i) v[i] = ld128(rws, o + (i >> 1) * MOD_STR + (i & 1) * 4608);
#pragma unroll
          for (int i = 0; i < 8; ++i) bad |= (v[i][1] ^ tag) | (v[i][3] ^ tag);
          if (__all(bad == 0) || (X_NOPOLL & 1)) break;
          __builtin_amdgcn_s_sleep(PSL);
        }
        STAMP(2);
        float m[4];
#pragma unroll
        for (int i = 0; i < 4; ++i) {
          const u32 e0 = v[i >> 1][2 * (i & 1)], e1 = v[2 + (i >> 1)][2 * (i & 1)], e2 = v[4 + (i >> 1)][2 * (i & 1)], e3 = v[6 + (i >> 1)][2 * (i & 1)];
          m[i] = rb((asf(e0) + asf(e1)) + (asf(e2) + asf(e3)));
        }
        const u32 hp = pk(rb(l0 * rb(1.f + m[2])) + m[0], rb(l1 * rb(1.f + m[3])) + m[1]);
        float a[8];
#pragma unroll
        for (int e = 0; e < 8; ++e) a[e] = 0.f;
#pragma unroll
        for (int p = 0; p < 9; ++p) {
          const u32 hb = nbc(hp, p);
#pragma unroll
          for (int e = 0; e < 8; ++e) a[e] = dot2(P[p][e], hb, a[e]);
        }
        float t[4];
#pragma unroll
        for (int i = 0; i < 4; ++i) t[i] = sw32(a[i], a[i + 4]);
        const u4 qo4 = {asu(sw16(t[0], t[2])), tag, asu(sw16(t[1], t[3])), tag};
        st128(qo4, rws, O_QP + pi * 1024 + lane * 16);
      }
      {
        int o = (X_NOPOLL & 2) ? O_END : O_QP + h * 8192 + lane * 16;
        u4 v[8];
        until(tz, TP2);
#pragma nounroll
        for (u32 n = 0; n < SPIN_G; ++n) {
          FENCE(); PIN(o);
          u32 bad = 0;
#pragma unroll
          for (int i = 0; i < 8; ++i) v[i] = ld128(rws, o + i * 1024);
#pragma unroll
          for (int i = 0; i < 8; ++i) bad |= (v[i][1] ^ tag) | (v[i][3] ^ tag);
          if (__all(bad == 0) || (X_NOPOLL & 2)) break;
          __builtin_amdgcn_s_sleep(PSL);
        }
        float q0 = 0.f, q1 = 0.f;
#pragma unroll
        for (int i = 0; i < 8; ++i) { const u32 u = v[i][0], w = v[i][2]; q0 += asf(u); q1 += asf(w); }
        sh->q[4 * (lane & 15) + g] = pk(q0, q1);
        lds_set(&sh->flag[3], lane);
      }
      STAMP(3);
      if (ffn && X_NOGU) {
      } else if (ffn) {
        GUP();
        u4 G[8];
        sfor<0, 8>([&](auto I) __attribute__((always_inline)) { if (I.value >= 2 * HW) VMCNT(2 * HW - 1); G[I.value] = LD_GU(I.value); SB(); });
        frag<4>(G, F);
        if (WDE) LD_WD(32 * D * 2, 4);
      } else if (b >= RED0) {
        const int pr = 8 * (b - RED0) + (lane & 7), vec = (lane >> 3) & 3;
        const u32 xq = __builtin_amdgcn_raw_buffer_load_b32(rx, pr * 4, 0, SMA);
        int o = O_MOD + (lane >> 5) * 2 * MOD_STR + (2 + vec) * 4608 + pr * 16;
        u4 v0, v1;
#pragma nounroll
        for (u32 n = 0; n < SPIN_G; ++n) {
          FENCE(); PIN(o);
          v0 = ld128(rws, o); v1 = ld128(rws, o + MOD_STR);
          if (__all(((v0[1] ^ tag) | (v0[3] ^ tag) | (v1[1] ^ tag) | (v1[3] ^ tag)) == 0)) break;
          __builtin_amdgcn_s_sleep(PSL);
        }
        const u32 a0 = v0[0], a1 = v0[2], b0 = v1[0], b1 = v1[2];
        const float m0 = asf(a0) + asf(b0), m1 = asf(a1) + asf(b1);
        const u32 d = pk(sw32(m0, m0), sw32(m1, m1));
        const u4 o4 = {d, tag, dppu<SHL + 1>(d), tag};
        if (lane >= 8 && lane < 24 && !(lane & 1)) st128(o4, rws, O_SC + ((vec - 1) * 288 + pr) * 8);
        if (lane < 32) sh->sc[lane] = vec == 1 ? xq : d;
      }
      float M;
      {
#pragma nounroll
        for (u32 n = 0; n < SPIN_L; ++n) {
          if (__all(((volatile LAS u32*)sh->flag)[8 + (lane & 7)] != 0)) break;
          __builtin_amdgcn_s_sleep(4);
        }
        FENCE();
        const f2 m = sh->ml[lane & 7];
        M = max8(m[0]);
        const float L = sum8(m[1] * __expf(m[0] - M));
        const u4 o = {asu(M), tag, asu(L), tag};
        if (lane == 0) st128(o, rws, O_ML + pi * 16);
        sh->gm = M;
        lds_set(&sh->flag[4], lane);
      }
      STAMP(4);
      lds_wait<8>(&sh->flag[6]);
      {
        int o = ffn ? O_SC + lane * 16 : O_END, o2 = O_ML + (8 * h + (lane & 7)) * 16;
        const u32 msk = ffn ? ~0u : 0u;
        u4 v[5], m;
#pragma nounroll
        for (u32 n = 0; n < SPIN_G; ++n) {
          FENCE(); PIN(o); PIN(o2);
          u32 bad = 0;
          m = ld128(rws, o2);
#pragma unroll
          for (int i = 0; i < 5; ++i) v[i] = ld128(rws, o + (i < 4 ? i * 1024 : 3584));
#pragma unroll
          for (int i = 0; i < 5; ++i) bad |= (v[i][1] ^ tag) | (v[i][3] ^ tag);
          if (__all(((bad & msk) | (m[1] ^ tag) | (m[3] ^ tag)) == 0)) break;
          __builtin_amdgcn_s_sleep(PSL);
        }
        if (ffn) {
#pragma unroll
          for (int i = 0; i < 5; ++i) { const u2 w = {v[i][0], v[i][2]}; *(LAS u2*)&sh->sc[(i < 4 ? 128 * i : 448) + 2 * lane] = w; }
        }
        const u32 mu = m[0], lu = m[2];
        const float Mh = max8(asf(mu));
        cw = __expf(M - Mh) / sum8(asf(lu) * __expf(asf(mu) - Mh));
      }
      STAMP(6);
    }
    if (CPF) asm volatile("" ::"v"(cpf));
#pragma unroll
    for (int t = 0; t < WK; ++t) asm volatile("" ::"v"(kb[t][0]));
  } else {
    const rsrc_t rv = mkrs(vc + slab, 262144);
    u4 vb[32], qv;
    float sp[32], s0 = 0.f, mw = 0.f, p0 = 0.f, p1 = 0.f, acc[8];
#pragma unroll
    for (int i = 0; i < 8; ++i) acc[i] = 0.f;
#define QK(i) do { float a_ = dot2(kb[i][0], qv[0], 0.f), b_ = dot2(kb[i][1], qv[1], 0.f); a_ = dot2(kb[i][2], qv[2], a_); b_ = dot2(kb[i][3], qv[3], b_); sp[i] = a_ + b_; PIN(sp[i]); } while (0)
#define PV(i) do { const u32 pb_ = nbc(asu((i) < 16 ? p0 : p1), (i) & 15), pl_ = pb_ >> 16; \
      _Pragma("unroll") for (int cc = 0; cc < 4; ++cc) { const u32 w_ = vb[i][cc]; acc[2 * cc] = dot2(w_, pl_, acc[2 * cc]); acc[2 * cc + 1] = dot2(w_, pb_, acc[2 * cc + 1]); } \
      _Pragma("unroll") for (int cc = 0; cc < 8; ++cc) PIN(acc[cc]); } while (0)
    sfor<WK, 64>([&](auto T) __attribute__((always_inline)) {
      constexpr int t = T.value;
      if constexpr (PS > 0 && t >= P0) {
        asm volatile("s_memrealtime vcc\n\ts_waitcnt vmcnt(%1)\n1:\n\ts_waitcnt lgkmcnt(0)\n\ts_sub_u32 vcc_lo, vcc_lo, %0\n\ts_cmpk_ge_u32 vcc_lo, %2\n\ts_cbranch_scc1 2f\n\ts_memrealtime vcc\n\ts_branch 1b\n2:"
                     :: "s"(tz), "n"(win(t) - 1), "n"(due(t)) : "memory", "vcc", "scc");
      } else {
        VMCNT(win(t) - 1);
      }
      if constexpr (t == WK) { if (CPF) asm volatile("" ::"v"(cpf)); STAMP(1); }
      if constexpr (t == 32) STAMP(4);
      if constexpr (t < 32) kb[t] = __builtin_amdgcn_raw_buffer_load_b128(rk, vo, t * 4096, NT);
      else vb[t - 32] = __builtin_amdgcn_raw_buffer_load_b128(rv, vo, (t - 32) * 4096, NT);
      SB();
      if constexpr (t == QS) {
        STAMP(2);
        lds_wait<1>(&sh->flag[3]);
        STAMP(3);
        qv = *(const LAS u4*)(sh->q + 4 * (lane & 15));
      }
      if constexpr (t >= QS && t < QS + 4) {
#pragma unroll
        for (int j = 8 * (t - QS); j < 8 * (t - QS) + 8; ++j) QK(j);
        if constexpr (t == QS + 1) { s0 = tred16(sp, lane); PIN(s0); }
      }
      if constexpr (t == QS + 4) {
        float s1 = tred16(sp + 16, lane);
        s0 = rb(rb(s0) * 0.08838834764831845f);
        s1 = rb(rb(s1) * 0.08838834764831845f);
        mw = wave_max(fmaxf(s0, s1));
        p0 = rb(__expf(s0 - mw)); p1 = rb(__expf(s1 - mw));
        const float lw = wave_sum(p0 + p1);
        const f2 w = {mw, lw};
        sh->ml[wave] = w;
        LDSW();
        *(volatile LAS u32*)&sh->flag[8 + wave] = 1;
        STAMP(5);
      }
      if constexpr (t > QS + 4) {
        constexpr int j0 = pv_done(t - 1), j1 = pv_done(t);
#pragma unroll
        for (int j = j0; j < j1; ++j) PV(j);
      }
      if constexpr (t == LATE) *(volatile LAS u32*)&sh->flag[6] = 1;
      if constexpr (t == 48) STAMP(6);
      SB();
    });
    if (GN) {
      GUP();
      G0 = LD_GU(0);
      if (GN > 1) G1 = LD_GU(1);
      if (GN > 2) G2 = LD_GU(2);
      if (GN > 3) G3 = LD_GU(3);
      SB();
    }
    lds_wait<1>(&sh->flag[4]);
    float fw = __expf(mw - sh->gm);
    tag = __builtin_amdgcn_readfirstlane(sh->tag);
    PIN(fw);
    constexpr int PD = pv_done(63);
#pragma unroll
    for (int j = PD; j < 32; ++j) { PV(j); SB(); }
    float t4[4];
#pragma unroll
    for (int i = 0; i < 4; ++i) t4[i] = sw32(acc[i], acc[i + 4]);
    const f2 o2 = {sw16(t4[0], t4[2]) * fw, sw16(t4[1], t4[3]) * fw};
    sh->ob[wave][lane + 8 * (lane >> 5)] = o2;
    STAMP(7);
    bar();
  }

  const int lane_ = lane;
  int pass = DRY ? (wave >> 3) ^ 1 : 1;
#pragma nounroll
  for (;;) {
    asm volatile("" : "+s"(pass));
    int lane = lane_;
    PIN(lane);
    const u32 nws = pass ? O_END : 0;
    const rsrc_t rw = mkrs(ws, nws);
    const u4 rs = {(u32)(size_t)ws, (u32)((size_t)ws >> 32) & 0xffffu, nws, 0x00020000u};
    const LAS f2* p = &sh->ob[0][((lane >> 1) & 7) + 8 * (lane >> 5) + 16 * (lane & 1) + 40 * ((lane >> 4) & 1)];
    int oo = O_OP + pi * 512 + lane * 8;
    PIN(p); PIN(oo);
    if (wave >= 8) STAMP(7);
    if (pass && wave >= 8) bar();
    STAMP(8);
    if (wave == 11) {
      f2 t[8];
#pragma unroll
      for (int i = 0; i < 8; ++i) t[i] = p[72 * i];
      const f2 r = (((t[0] + t[1]) + (t[2] + t[3])) + ((t[4] + t[5]) + (t[6] + t[7]))) * cw;
      const float r0 = r[0], r1 = r[1];
      const u2 o = {pk(r0, r1), tag};
      __builtin_amdgcn_raw_buffer_store_b64(o, rw, oo, 0, STA);
      STAMP(9);
    }
    if (ffn) {
      const int f = b - 112;
      GUP();
      if (pass) bar();
      if (wave < 5) {
        u4 G[8];
        if (GN > 0) G[0] = G0;
        if (GN > 1) G[1] = G1;
        if (GN > 2) G[2] = G2;
        if (GN > 3) G[3] = G3;
        sfor<GN, 8>([&](auto I) __attribute__((always_inline)) { G[I.value] = LD_GU(I.value); });
        SB();
        frag<4>(G, F);
      } else if (!WDE && wave > 8) {
        LD_WD(pass ? 32 * D * 2 : 0, 0);
      }
      int fo = O_FC + ((12 * (wave - 9) + ((lane & 15) >> 2)) * 144 + f) * 128 + (lane & 3) * 16;
      const LAS float* gq = sh->gp[32 * ((lane >> 3) & 1) + 8 * (lane >> 4) + (lane & 7)];
      if (unit < 9) {
        const int par = (lane >> 2) & 1;
        LAS float* gp = &sh->gp[32 * ((lane >> 3) & 1) + 8 * (lane & 3)][2 * unit + par];
        const LAS f2* stp = &sh->st[lane & 15];
        PIN(fo); PIN(gq); PIN(gp); PIN(stp);
        int o1 = O_X2 + (f & 15) * 4608 + (4 * unit + (lane >> 4)) * 128 + ((lane & 15) >> 1) * 8;
        const float sbv = lo16(((const LAS u16*)sh->sc)[64 * unit + lane]);
        float cbv = rb(1.f + lo16(((const LAS u16*)sh->sc)[D + 64 * unit + lane]));
        PIN(cbv);
        STAMPT(10);
        u2 xr;
#pragma nounroll
        for (u32 n = 0; n < SPIN_G; ++n) {
          FENCE(); PIN(o1);
          xr = ld64(rw, o1);
          if (__all(xr[1] == tag) || !pass) break;
        }
        STAMPT(11);
        const u32 x0 = xr[0];
        const float xv = (lane & 1) ? hi16(x0) : lo16(x0);
        { const f2 w = {wave_sum(xv), wave_sum(xv * xv)}; sh->st[unit] = w; }
        if (pass) bar();
        const f2 st = *stp;
        const float mean = row_sum(st[0]) * (1.f / D), e2 = row_sum(st[1]) * (1.f / D);
        const float rsd = __builtin_amdgcn_rsqf(fmaxf(fmaf(-mean, mean, e2), 0.f) + 1e-5f);
        const u4 a = gather8(fmaf(xv - mean, rsd * cbv, sbv));
        f4 dd[8];
#pragma unroll
        for (int j = 0; j < 8; ++j) { f4 z = {0.f, 0.f, 0.f, 0.f}; dd[j] = mfma(a, F[j], z); }
        if ((lane >> 4) == 2 * par) {
#pragma unroll
          for (int j = 0; j < 8; ++j) gp[20 * j] = dd[j][0];
        }
        if (pass) bar();
        STAMPT(12);
      } else {
        bar(); bar();
      }
      if (wave > 8) {
        f4 t[5];
#pragma unroll
        for (int i = 0; i < 5; ++i) t[i] = *(const LAS f4*)(gq + 4 * i);
        const f4 tt = (t[0] + t[1]) + (t[2] + t[3]) + t[4];
        const float gs = (tt[0] + tt[1]) + (tt[2] + tt[3]);
        const float us = dpp<SHL + 8>(gs);
        const u4 a = gather8(silu(gs) * us);
#pragma unroll
        for (int e = 0; e < 3; ++e) {
          float r[4];
#pragma unroll
          for (int j = 0; j < 4; ++j) { f4 z = {0.f, 0.f, 0.f, 0.f}; const f4 t4 = mfma(a, F[8 + 4 * e + j], z); r[j] = t4[0]; }
          const u4 o = {pk(r[0], r[1]), tag, pk(r[2], r[3]), tag};
          if (lane < 16) st128(o, rw, fo + e * 4 * 144 * 128);
        }
        STAMPT(13);
      }
    } else if (wave == 11) {
      if (b >= RED0) {
        const int rho = b - RED0;
        const u2 ga = *(const LAS u2*)&sh->sc[2 * (lane & 3)], xi = *(const LAS u2*)&sh->sc[8 + 2 * (lane & 3)], gb = *(const LAS u2*)&sh->sc[24 + 2 * (lane & 3)];
        const u32 one = (lane & 1) ? 0x3f800000u : 0x3f80u;
        u4 sel = {(lane & 2) ? 0u : one, 0u, (lane & 2) ? one : 0u, 0u};
        PIN(sel);
        const u32 ga0 = ga[0], ga1 = ga[1], xi0 = xi[0], xi1 = xi[1], gb0 = gb[0], gb1 = gb[1];
        u4 v[9];
        int xo = O_X2 + (lane >> 2) * 4608 + rho * 128 + (lane & 3) * 16, co = O_FC + (rho * 144 + (lane >> 2)) * 128 + (lane & 3) * 16;
        PIN(xo); PIN(co);
        poll<2, 2048, 0>(v, rs, O_YA + (rho * 32 + (lane >> 2)) * 128 + (lane & 3) * 16, tag, pass);
        STAMPT(10);
        const f4 y = rec_sum<2>(v, sel);
        const u32 d0 = pk(fmaf(lo16(ga0), y[0], lo16(xi0)), fmaf(hi16(ga0), y[1], hi16(xi0)));
        const u32 d1 = pk(fmaf(lo16(ga1), y[2], lo16(xi1)), fmaf(hi16(ga1), y[3], hi16(xi1)));
        st128(u4{d0, tag, d1, tag}, rw, xo);
        STAMPT(11);
        if (rho == 0 && lane == 0) __builtin_amdgcn_raw_buffer_store_b32(tag, rw, O_EPOCH, 0, STA);
        poll<9, 2048, 0>(v, rs, co, tag, pass);
        STAMPT(12);
        const f4 r = rec_sum<9>(v, sel);
        const u2 o = {pk(fmaf(lo16(gb0), r[0], lo16(d0)), fmaf(hi16(gb0), r[1], hi16(d0))), pk(fmaf(lo16(gb1), r[2], lo16(d1)), fmaf(hi16(gb1), r[3], hi16(d1)))};
        __builtin_amdgcn_raw_buffer_store_b64(o, mkrs(out + 16 * rho, pass ? 32 : 0), lane * 8, 0, 0);
        STAMPT(13);
      }
    } else if (wave >= 8 && b < 96) {
      const int hd = b & 31;
      int yo = O_YA + ((4 * (3 * (b >> 5) + wave - 8) + ((lane & 7) >> 1)) * 32 + hd) * 128 + (2 * (lane & 1) + (lane >> 5)) * 16;
      PIN(yo);
      u4 v[4];
      poll<4, 128, 0>(v, rs, O_OP + (8 * hd + ((lane & 15) >> 1)) * 512 + (2 * (lane >> 4) + (lane & 1)) * 16, tag, pass);
      STAMPT(10);
      const u4 a0 = {v[0][0], v[0][2], v[1][0], v[1][2]}, a1 = {v[2][0], v[2][2], v[3][0], v[3][2]};
      float w[8], t[4];
#pragma unroll
      for (int j = 0; j < 8; ++j) {
        const f4 z = {0.f, 0.f, 0.f, 0.f};
        const f4 d = mfma(a1, F[8 + j], mfma(a0, F[j], z));
        const f2 e = d.xy + d.zw;
        const float e0 = e[0], e1 = e[1];
        w[j] = e0 + dpp<ROR8>(e1);
      }
#pragma unroll
      for (int i = 0; i < 4; ++i) t[i] = sw32(w[i], w[i + 4]);
      u32 r0, r1;
      swap16(pk(sw16(t[0], t[2]), sw16(t[1], t[3])), r0, r1);
      if ((lane & 24) == 0) st128(u4{r0, tag, r1, tag}, rw, yo);
      STAMPT(11);
    }
    if (pass) break;
    pass = 1;
  }
  if (GN > 0) asm volatile("" ::"v"(G0[0]));
  if (GN > 1) asm volatile("" ::"v"(G1[0]));
  if (GN > 2) asm volatile("" ::"v"(G2[0]));
  if (GN > 3) asm volatile("" ::"v"(G3[0]));
  STAMP(15);
  STAMP_OUT();
}

extern "C" void kernel_launch(void* const* d_in, const int* in_sizes, int n_in, void* d_out, int out_size, void* d_ws, size_t ws_size,
                              hipStream_t stream) {
  fused_kernel<<<256, 768, 0, stream>>>((const u16*)d_in[0], (const u16*)d_in[1], (const u16*)d_in[2], (const u16*)d_in[3],
                                        (const u16*)d_in[4], (const u16*)d_in[6], (const u16*)d_in[7], (const u16*)d_in[8],
                                        (const u16*)d_in[9], (const u16*)d_in[10], (u16*)d_out, (char*)d_ws + WSO(ws_size));
}
```

```cpp
#include <hip/hip_runtime.h>
#include <stdint.h>

#define STAMPS 0
#define MAPB 0
#define PS 0
#define P0 8
#define POFF 80
#define PKNOTS 90, 190, 350, 605, 900, 1145, 1415, 1650, 1880, 2105
#define X_NOWO 0
#define X_NOPOLL 0
#define PRIO 0
#define WSE 0
#define WSO(n) (WSE ? ((n) - (size_t)WSE * 1048576) & ~(size_t)0xfffff : 0)
#define YSL 12
#define ASL 8
#define FDL 45
#define DPO 1
#define DPY 3
#define DPS 2
#define DPF 3
#define GPO (22 / DPO)
#define GPY 5
#define GPS 8
#define GPF 5
#define OTH 3
#define ATH 6
#define SDL 16
#define PW 0
#define PLN 0
#define PLM "sc1"
#define STM 16
#define PR 0
#define YDL 44
#define ODL 0
#define STR_ 16
#define STF (STR_ == 16 ? 512 : 18 * STR_)
#define PE 0
#define GW 8
#define RMAP 0
#define ROLE(x) (RMAP ? ((x) & 7) * 32 + ((x) >> 3) : (x))
#define GE 96
#define GT 44
#define WDW 1
#define X_A 0
#define WK 3
#define WV 4
#define T4 64
#define PV0 4
#define GN 2
#define WQW 18
#define LATE 54
#define CPF 21200
#define PSL 16
#define TP1 700
#define TP2 1000
#define HW 1

typedef unsigned u32;
typedef unsigned long long u64;
typedef unsigned short u16;
typedef u32 u1 __attribute__((ext_vector_type(1)));
typedef u32 u2 __attribute__((ext_vector_type(2)));
typedef u32 u3 __attribute__((ext_vector_type(3)));
typedef u32 u4 __attribute__((ext_vector_type(4)));
typedef float f2 __attribute__((ext_vector_type(2)));
typedef float f4 __attribute__((ext_vector_type(4)));
typedef __bf16 bf2 __attribute__((ext_vector_type(2)));
typedef __bf16 bf8 __attribute__((ext_vector_type(8)));
typedef __amdgpu_buffer_rsrc_t rsrc_t;
#define LAS __attribute__((address_space(3)))
#define GAS __attribute__((address_space(1)))
#define DI __device__ __forceinline__

constexpr int D = 576, FF = 4608;
constexpr int SC1 = 16, NT = 2;
constexpr u32 SPIN_G = 1u << 17, SPIN_L = 1u << 20;

constexpr int O_EPOCH = 0;
constexpr int O_ML = 12288;
constexpr int O_MOD = 16384, MOD_STR = 3456 * 8;
constexpr int O_YA = 131072;
constexpr int O_QP = 262144;
constexpr int O_OP = O_QP + 262144;
constexpr int O_FC = O_OP + 131072;
constexpr int O_S = O_FC + 18 * 144 * 128;
constexpr int O_AB = O_S + 144 * 18 * 512;
constexpr int O_ST = O_AB + 144 * 18 * 512;
constexpr int O_END = O_ST + 144 * 18 * 128;
constexpr int O_STAMP = 4 << 20;
constexpr int S1 = 96, S2 = 112;

DI float asf(u32 x) { return __uint_as_float(x); }
DI u32 asu(float x) { return __float_as_uint(x); }
DI u32 pk(float lo, float hi) { f2 t = {lo, hi}; bf2 r = __builtin_convertvector(t, bf2); return __builtin_bit_cast(u32, r); }
DI float rb(float x) { return asf(pk(0.f, x)); }
DI float lo16(u32 x) { return asf(x << 16); }
DI float hi16(u32 x) { return asf(x & 0xffff0000u); }
template <int C> DI u32 dppu(u32 x) { return (u32)__builtin_amdgcn_update_dpp(0, (int)x, C, 0xf, 0xf, true); }
template <int C> DI float dpp(float x) { return asf(dppu<C>(asu(x))); }
constexpr int ROR4 = 0x124, ROR8 = 0x128, HMIR = 0x141, QX1 = 0xB1, QX2 = 0x4E, SHL = 0x100, NBC = 0x150;
DI u32 nbc(u32 src, int n) {
  switch (n) {
#define CASE(n) case n: return dppu<NBC + n>(src);
    CASE(0) CASE(1) CASE(2) CASE(3) CASE(4) CASE(5) CASE(6) CASE(7) CASE(8) CASE(9) CASE(10) CASE(11) CASE(12) CASE(13) CASE(14)
#undef CASE
    default: return dppu<NBC + 15>(src);
  }
}
DI float dot2(u32 a, u32 b, float c) { return __builtin_amdgcn_fdot2_f32_bf16(__builtin_bit_cast(bf2, a), __builtin_bit_cast(bf2, b), c, false); }
DI float sw32(float a, float b) { auto r = __builtin_amdgcn_permlane32_swap(asu(a), asu(b), false, false); u32 r0 = r[0], r1 = r[1]; return asf(r0) + asf(r1); }
DI float sw16(float a, float b) { auto r = __builtin_amdgcn_permlane16_swap(asu(a), asu(b), false, false); u32 r0 = r[0], r1 = r[1]; return asf(r0) + asf(r1); }
DI float mx32(float a) { auto r = __builtin_amdgcn_permlane32_swap(asu(a), asu(a), false, false); u32 r0 = r[0], r1 = r[1]; return fmaxf(asf(r0), asf(r1)); }
DI float mx16(float a) { auto r = __builtin_amdgcn_permlane16_swap(asu(a), asu(a), false, false); u32 r0 = r[0], r1 = r[1]; return fmaxf(asf(r0), asf(r1)); }
DI float row_sum(float v) { v += dpp<QX1>(v); v += dpp<QX2>(v); v += dpp<ROR4>(v); return v + dpp<ROR8>(v); }
DI float wave_sum(float v) { v = row_sum(v); v = sw16(v, v); return sw32(v, v); }
DI float wave_max(float v) {
  v = fmaxf(v, dpp<QX1>(v)); v = fmaxf(v, dpp<QX2>(v)); v = fmaxf(v, dpp<ROR4>(v)); v = fmaxf(v, dpp<ROR8>(v));
  return mx32(mx16(v));
}
DI float sum8(float v) { v += dpp<QX1>(v); v += dpp<QX2>(v); return v + dpp<HMIR>(v); }
DI float max8(float v) { v = fmaxf(v, dpp<QX1>(v)); v = fmaxf(v, dpp<QX2>(v)); return fmaxf(v, dpp<HMIR>(v)); }
DI u32 plo(u32 a, u32 b) { return __builtin_amdgcn_perm(b, a, 0x05040100u); }
DI u32 phi(u32 a, u32 b) { return __builtin_amdgcn_perm(b, a, 0x07060302u); }
DI f4 mfma(u4 a, u4 b, f4 c) { return __builtin_amdgcn_mfma_f32_16x16x32_bf16(__builtin_bit_cast(bf8, a), __builtin_bit_cast(bf8, b), c, 0, 0, 0); }
DI rsrc_t mkrs(const void* p, u32 n) { return __builtin_amdgcn_make_buffer_rsrc((void*)p, 0, n, 0x00020000); }
#define FENCE() asm volatile("" ::: "memory")
#define SB() __builtin_amdgcn_sched_barrier(0)
#define PIN(v) asm volatile("" : "+v"(v))
#define VMCNT(n) asm volatile("s_waitcnt vmcnt(%0)" ::"n"(n) : "memory")
#define LDSW() asm volatile("s_waitcnt lgkmcnt(0)" ::: "memory")
DI void bar() { asm volatile("s_waitcnt lgkmcnt(0)\n\ts_barrier" ::: "memory"); }
DI float silu(float v) { return v * __builtin_amdgcn_rcpf(1.f + __expf(-v)); }
DI u4 ld128(rsrc_t r, int o) { return __builtin_amdgcn_raw_buffer_load_b128(r, o, 0, SC1); }
DI u2 ld64(rsrc_t r, int o) { return __builtin_amdgcn_raw_buffer_load_b64(r, o, 0, SC1); }
DI void st128(u4 v, rsrc_t r, int o) { __builtin_amdgcn_raw_buffer_store_b128(v, r, o, 0, STM); }
DI float ln_rstd(float var) { return __builtin_amdgcn_rcpf(rb(__builtin_amdgcn_sqrtf(rb(rb(var) + 1e-5f)))); }

struct __attribute__((aligned(16))) Sh {
  f2 ob[8][72];
  float mp[3][64];
  u32 am[7][16];
  u32 q[64];
  f2 ml[8];
  float gm;
  u32 tag;
  u32 pad[2];
  u32 flag[16];
};

#define STAMP(i)
#define STAMP_OUT()

#define STAMPT(i) STAMP(i)
#define STAMPK(i)
template <int S> DI void lds_wait(volatile LAS u32* f) {
#pragma nounroll
  for (u32 n = 0; n < SPIN_L; ++n) { if (*f != 0) break; __builtin_amdgcn_s_sleep(S); }
  FENCE();
}
DI void until(u32 tz, u32 t) {
#pragma nounroll
  for (u32 n = 0; n < 4096; ++n) { if ((u32)__builtin_amdgcn_s_memrealtime() - tz >= t) break; __builtin_amdgcn_s_sleep(2); }
}
DI void lds_set(LAS u32* f, int lane) { LDSW(); if (lane == 0) *(volatile LAS u32*)f = 1; }

DI float tred16(const float* s, int lane) {
  float t[8], u[4], v[2];
  const bool b3 = lane & 8, b2 = lane & 4, b1 = lane & 2, b0 = lane & 1;
#pragma unroll
  for (int i = 0; i < 8; ++i) t[i] = (b3 ? s[i + 8] : s[i]) + dpp<ROR8>(b3 ? s[i] : s[i + 8]);
#pragma unroll
  for (int i = 0; i < 4; ++i) u[i] = (b2 ? t[i + 4] : t[i]) + dpp<HMIR>(b2 ? t[i] : t[i + 4]);
#pragma unroll
  for (int i = 0; i < 2; ++i) v[i] = (b1 ? u[i + 2] : u[i]) + dpp<QX2>(b1 ? u[i] : u[i + 2]);
  return (b0 ? v[1] : v[0]) + dpp<QX1>(b0 ? v[0] : v[1]);
}

template <int C, typename V> DI void frag(const V* L, u4* F) {
#pragma unroll
  for (int c = 0; c < C; ++c) {
    u4 a, b;
#pragma unroll
    for (int j = 0; j < 4; ++j) { const u32 x = L[2 * j][c], y = L[2 * j + 1][c]; a[j] = plo(x, y); b[j] = phi(x, y); }
    PIN(a); PIN(b);
    F[2 * c] = a; F[2 * c + 1] = b;
  }
}

DI u4 gather8(float v) {
  const u32 d = pk(v, dpp<SHL + 1>(v));
  u4 a; a[0] = d; a[1] = dppu<SHL + 2>(d); a[2] = dppu<SHL + 4>(d); a[3] = dppu<SHL + 6>(d);
  return a;
}

template <int I> struct IC { static constexpr int value = I; };
template <int I, int N, class F> DI void sfor(F&& f) { if constexpr (I < N) { f(IC<I>{}); sfor<I + 1, N>(f); } }

DI void swap16(u32 d, u32& r0, u32& r1) { auto r = __builtin_amdgcn_permlane16_swap(d, d, false, false); r0 = r[0]; r1 = r[1]; }

template <int O> DI void mload(u4& v, u4 rs, int off, u64 m) {
  asm volatile("s_mov_b64 exec, %2\n\tbuffer_load_dwordx4 %0, %1, %3, %4 offen offset:%5 sc1\n\ts_mov_b64 exec, -1" : "+v"(v) : "v"(off), "s"(m), "s"(rs), "s"(O & ~4095), "n"(O & 4095) : "memory");
}
DI u64 missing(u4& v, u32 tag) { PIN(v); const u32 t1 = v[1], t3 = v[3]; return __ballot(t1 != tag) | __ballot(t3 != tag); }
template <int N, int STR, int SL> DI void poll(u4* v, u4 rs, int off, u32 tag) {
  u64 m[N], any;
#pragma unroll
  for (int i = 0; i < N; ++i) { m[i] = ~0ull; asm volatile("" : "=v"(v[i])); }
  int n = SPIN_G;
#pragma nounroll
  do {
    sfor<0, N>([&](auto I) __attribute__((always_inline)) { mload<I.value * STR>(v[I.value], rs, off, m[I.value]); });
    VMCNT(0);
    any = 0;
#pragma unroll
    for (int i = 0; i < N; ++i) { m[i] = missing(v[i], tag); any |= m[i]; }
    if (--n < 0) any = 0;
    if (SL && any) __builtin_amdgcn_s_sleep(SL);
  } while (any);
}
typedef u32 u16v __attribute__((ext_vector_type(16)));
struct Land { u16v a, b, c; };
#define PREG(d, i, e) "v[108+16*" #d "+4*" #i "+" #e "]"
#define PLD(d, i, o) "s_mov_b64 exec, %[m" #i "]\n\tbuffer_load_dwordx4 v[108+16*" #d "+4*" #i ":111+16*" #d "+4*" #i "], %[" #o "], %[rs], 0 offen offset:%[i" #i "] " PLM "\n\t"
#define PMG(d, i) "s_mov_b64 exec, %[m" #i "]\n\tv_cmpx_eq_u32_e32 vcc, %[tag], " PREG(d, i, 1) "\n\tv_cmpx_eq_u32_e32 vcc, %[tag], " PREG(d, i, 3) "\n\t" \
  "v_mov_b32 %[a" #i "], " PREG(d, i, 0) "\n\tv_mov_b32 %[b" #i "], " PREG(d, i, 2) "\n\ts_andn2_b64 %[m" #i "], %[m" #i "], exec\n\t"
#define PI4(d) PLD(d, 0, o) PLD(d, 1, o) PLD(d, 2, o) PLD(d, 3, p)
#define PM4(d) PMG(d, 0) PMG(d, 1) "s_or_b64 %[t], %[m0], %[m1]\n\t" PMG(d, 2) "s_or_b64 %[t], %[t], %[m2]\n\t" PMG(d, 3) "s_or_b64 %[t], %[t], %[m3]\n\t"
#define PSLOW ".if %[sl]>0\n3:\n\t" PI4(0) "s_waitcnt vmcnt(0)\n\t" PM4(0) "s_cbranch_scc0 2f\n\t" \
  "s_bcnt1_i32_b64 vcc_lo, %[m0]\n\ts_bcnt1_i32_b64 vcc_hi, %[m1]\n\ts_add_u32 vcc_lo, vcc_lo, vcc_hi\n\ts_bcnt1_i32_b64 vcc_hi, %[m2]\n\ts_add_u32 vcc_lo, vcc_lo, vcc_hi\n\t" \
  "s_bcnt1_i32_b64 vcc_hi, %[m3]\n\ts_add_u32 vcc_lo, vcc_lo, vcc_hi\n\ts_cmpk_le_u32 vcc_lo, %[thr]\n\ts_cbranch_scc1 4f\n\ts_sleep %[sl]\n\t" \
  "s_sub_u32 %[n], %[n], 1\n\ts_cbranch_scc0 3b\n\ts_branch 2f\n4:\n\t.endif\n\t"
#define PR4(d) "s_waitcnt vmcnt(%[w])\n\t" PM4(d) "s_cbranch_scc0 2f\n\t" PI4(d)
#define PI2(d) PLD(d, 0, o) PLD(d, 1, o)
#define PR2(d) "s_waitcnt vmcnt(%[w])\n\t" PMG(d, 0) PMG(d, 1) "s_or_b64 %[t], %[m0], %[m1]\n\ts_cbranch_scc0 2f\n\t" PI2(d)
#define PEND "s_sub_u32 %[n], %[n], 1\n\ts_cbranch_scc0 1b\n2:\n\ts_mov_b64 exec, -1"
#define PLOOP(I, R) I(0) ".if %[dp]>1\n\ts_sleep %[gap]\n\t" I(1) ".endif\n\t.if %[dp]>2\n\ts_sleep %[gap]\n\t" I(2) ".endif\n1:\n\t" \
  R(0) ".if %[dp]>1\n\t" R(1) ".endif\n\t.if %[dp]>2\n\t" R(2) ".endif\n\t" PEND
#define PLAND "=&{v[108:123]}"(l.a), "=&{v[124:139]}"(l.b), "=&{v[140:155]}"(l.c)
#define POUT4 [a0] "+v"(r[0]), [b0] "+v"(r[1]), [a1] "+v"(r[2]), [b1] "+v"(r[3]), [a2] "+v"(r[4]), [b2] "+v"(r[5]), [a3] "+v"(r[6]), [b3] "+v"(r[7]), \
  [m0] "+s"(m0), [m1] "+s"(m1), [m2] "+s"(m2), [m3] "+s"(m3), [t] "=&s"(t), [n] "+s"(n)
template <int DP, int GAP, int STR, int THR, int SL> DI void ppoll4(Land& l, u32* r, u4 rs, int o, u32 tag) {
  u64 m0 = ~0ull, m1 = ~0ull, m2 = ~0ull, m3 = ~0ull, t;
  u32 n = SPIN_G;
  asm volatile(PSLOW PLOOP(PI4, PR4) : PLAND, POUT4
    : [o] "v"(o), [p] "v"(o), [rs] "s"(rs), [tag] "s"(tag), [i0] "n"(0), [i1] "n"(STR), [i2] "n"(2 * STR), [i3] "n"(3 * STR), [w] "n"(4 * DP - 4), [dp] "n"(DP), [gap] "n"(GAP),
      [thr] "n"(THR), [sl] "n"(SL)
    : "memory", "vcc", "scc");
}
#define QLD(i) "s_mov_b64 exec, %[k" #i "]\n\tbuffer_load_dwordx4 v[140+4*" #i ":143+4*" #i "], %[o], %[rs], %[ao] offen offset:%[i" #i "] " PLM "\n\t"
#define QCK(i) "s_mov_b64 exec, %[k" #i "]\n\tv_cmpx_eq_u32_e32 vcc, %[tag], " PREG(2, i, 1) "\n\tv_cmpx_eq_u32_e32 vcc, %[tag], " PREG(2, i, 3) "\n\t" \
  "s_nop 0\n\ts_andn2_b64 %[k" #i "], %[k" #i "], exec\n\t"
template <int DL, int GAP, int AO> DI void ppoll7(Land& l, u32* r, u4 rs, int o, int p, u64 m3, u32 tag) {
  u64 m0 = ~0ull, m1 = (X_A & 2) ? 0 : ~0ull, m2 = m1, k0 = (X_A & 5) ? 0 : ~0ull, k1 = k0, k2 = k0, t, u;
  u32 n = SPIN_G;
  asm volatile(QLD(0) QLD(1) QLD(2) "s_sleep %[dl]\n\t" PI4(0) "s_sleep %[gap]\n\t" PI4(1) "\n1:\n\t"
    "s_waitcnt vmcnt(8)\n\t" QCK(0) QCK(1) QCK(2) "s_or_b64 %[u], %[k0], %[k1]\n\ts_or_b64 %[u], %[u], %[k2]\n\t"
    "s_waitcnt vmcnt(4)\n\t" PM4(0) "s_or_b64 %[t], %[t], %[u]\n\ts_cbranch_scc0 2f\n\t" QLD(0) QLD(1) QLD(2) PI4(0)
    "s_waitcnt vmcnt(7)\n\t" PM4(1) "s_or_b64 %[t], %[t], %[u]\n\ts_cbranch_scc0 2f\n\t" PI4(1) PEND
    : PLAND, POUT4, [k0] "+s"(k0), [k1] "+s"(k1), [k2] "+s"(k2), [u] "=&s"(u)
    : [o] "v"(o), [p] "v"(p), [rs] "s"(rs), [tag] "s"(tag), [ao] "s"(AO), [i0] "n"(0), [i1] "n"(1024), [i2] "n"(2048), [i3] "n"(0), [dl] "n"(DL), [gap] "n"(GAP)
    : "memory", "vcc", "scc");
}
template <int DP, int GAP, int STR> DI void ppoll2(Land& l, u32* r, u4 rs, int o, u32 tag) {
  u64 m0 = ~0ull, m1 = ~0ull, t;
  u32 n = SPIN_G;
  asm volatile(PLOOP(PI2, PR2)
    : PLAND, [a0] "+v"(r[0]), [b0] "+v"(r[1]), [a1] "+v"(r[2]), [b1] "+v"(r[3]), [m0] "+s"(m0), [m1] "+s"(m1), [t] "=&s"(t), [n] "+s"(n)
    : [o] "v"(o), [rs] "s"(rs), [tag] "s"(tag), [i0] "n"(0), [i1] "n"(STR), [w] "n"(2 * DP - 2), [dp] "n"(DP), [gap] "n"(GAP)
    : "memory", "vcc", "scc");
}
DI void drain(const Land& l) { asm volatile("s_waitcnt vmcnt(0)" :: "{v[108:123]}"(l.a), "{v[124:139]}"(l.b), "{v[140:155]}"(l.c) : "memory"); }
DI u4 cat(const u32* r) { return u4{r[0], r[1], r[2], r[3]}; }
DI f4 fold8(f4 d) {
  f4 r;
#pragma unroll
  for (int i = 0; i < 4; ++i) { const float t = d[i]; r[i] = t + dpp<ROR8>(t); }
  return r;
}
DI u4 selector(int lane) {
  const u32 one = (lane & 1) ? 0x3f800000u : 0x3f80u, a = (lane & 2) ? 0u : one, b = (lane & 2) ? one : 0u;
  return u4{a, b, a, b};
}
DI f4 last_sum(Land& l, u4 rs, int r, int w, int lane, u32 tag) {
  u32 v[4] = {};
  u4 sel = selector(lane);
  int o = O_FC + (r * 144 + 16 * w) * 128 + lane * 16;
  PIN(sel); PIN(o);
  if (FDL) __builtin_amdgcn_s_sleep(FDL);
  ppoll2<DPF, GPF, 1024>(l, v, rs, o, tag);
  const f4 z = {0.f, 0.f, 0.f, 0.f};
  return mfma(sel, cat(v), z);
}
DI u4 pick(u32 t0, u32 t1, u32 t2, u32 t3, u32 t4, u32 t5, u32 t6, u32 t7, int lane, u32 tag) {
  const bool b4 = lane & 16, b5 = lane & 32;
  const u32 a0 = b4 ? t2 : t0, a1 = b4 ? t3 : t1, c0 = b4 ? t6 : t4, c1 = b4 ? t7 : t5;
  return u4{b5 ? c0 : a0, tag, b5 ? c1 : a1, tag};
}
#define PICK(t) pick(t[0], t[1], t[2], t[3], t[4], t[5], t[6], t[7], lane, tag)

DI rsrc_t gu_rs(const u16* wg, const u16* wu, int cb, int rb, int w, bool on) {
  return mkrs((w < 4 ? wg : wu) + (on ? 32 * rb * FF + 512 * cb + 128 * (w & 3) : 0), on ? 31 * FF * 2 + 256 : 0);
}
#define GU_TAIL(cb, w) { \
      int so = O_S + ((16 * (cb) + 4 * ((w) & 3) + (lane >> 4)) * 18 + rb1) * 512 + ((w) >> 2) * 256 + (lane & 15) * 16; \
      const LAS u4* ap = (const LAS u4*)&sh->am[lane & 3][4 * (lane >> 4)]; \
      PIN(so); PIN(ap); \
      u4 a = *ap, F[8]; \
      frag<4>(G, F); \
      STAMPT(10); \
      const f4 z = {0.f, 0.f, 0.f, 0.f}; \
      u32 t[8]; \
      _Pragma("unroll") for (int i = 0; i < 8; ++i) { const f4 d = mfma(a, F[i], z); t[i] = pk(d[2], d[3]); } \
      __builtin_amdgcn_raw_buffer_store_b128(PICK(t), rws, so, O_AB - O_S, SC1); \
      STAMPT(11); \
      bar();                                                             \
      STAMPT(12); \
      a = *ap; \
      _Pragma("unroll") for (int i = 0; i < 8; ++i) { const f4 d = mfma(a, F[i], z); t[i] = asu(d[0] + d[1]); } \
      st128(PICK(t), rws, so); \
      STAMPT(13); }
constexpr int win(int t) { return t >= T4 || (PS > 0 && t >= P0) ? WV : WK; }
constexpr int due(int t) {
  constexpr int kt[] = {3, 6, 10, 16, 24, 32, 40, 48, 56, 64}, kv[] = {PKNOTS};
  int i = 0;
  while (kt[i + 1] <= t) ++i;
  return POFF + (kv[i] + (kv[i + 1] - kv[i]) * (t - kt[i]) / (kt[i + 1] - kt[i])) * PS / 1000;
}
constexpr int QS = 32 + PV0 - 1;
constexpr int pv_done(int t) {
  int n = 0;
  for (int i = QS + 5; i <= t; ++i) { const int av = i - 32 - win(i) + 1; n = n + 3 < av ? n + 3 : (av > n ? av : n); }
  return n;
}

__global__ void __launch_bounds__(768, 3)
fused_kernel(const u16* x, const u16* kc, const u16* vc, const u16* wq, const u16* wo, const u16* wg, const u16* wu, const u16* wd,
             const u16* cv, const u16* wmod, u16* out, char* ws) {
  __shared__ Sh shm;
  LAS Sh* sh = (LAS Sh*)&shm;
  STAMP(0);
  const int lane = threadIdx.x & 63, wave = __builtin_amdgcn_readfirstlane(threadIdx.x >> 6);
  const int bx = blockIdx.x, xc = bx & 7, ci = bx >> 3, b = ROLE(bx), s = MAPB ? xc : ci & 7, h = MAPB ? ci : (ci >> 3) * 8 + xc, pi = 8 * h + s;
  u32 cpf = 0;
  if (CPF) cpf = __builtin_amdgcn_raw_buffer_load_b32(mkrs((const void*)(__builtin_amdgcn_s_getpc() & ~255ull), CPF), (((ci & 3) * 64 + lane) * 128) | (wave == 11 ? 0 : 1 << 30), 0, 0);
  const u32 tz = (u32)__builtin_amdgcn_s_memrealtime();
  SB();
  const size_t slab = ((size_t)h * 8192 + s * 1024) * 128;
  const int vo = (wave >> 2) * 131072 + (wave & 3) * 1024 + lane * 16;
  asm volatile("" : "+s"(x), "+s"(kc), "+s"(vc), "+s"(wq), "+s"(wo), "+s"(wg), "+s"(wu), "+s"(wd), "+s"(cv), "+s"(wmod), "+s"(out), "+s"(ws));
  const rsrc_t rk = mkrs(kc + slab, 262144);
  u4 kb[32];
#pragma unroll
  for (int t = 0; t < WK; ++t) kb[t] = __builtin_amdgcn_raw_buffer_load_b128(rk, vo, t * 4096, NT);
  SB();
  if (threadIdx.x < 16) sh->flag[threadIdx.x] = 0;
  bar();
  const int cb1 = b < S2 ? 1 + (b & 7) : (b - S2) >> 4, rb1 = b < S1 ? b : b < S2 ? (b >> 3) & 1 : 2 + (b & 15);
  const rsrc_t rws = mkrs(ws, O_END);
  const u4 rs = {(u32)(size_t)ws, (u32)((size_t)ws >> 32) & 0xffffu, O_END, 0x00020000u};

  if (__builtin_expect(wave >= 8, 1)) {
    STAMPK(10);
    if (__builtin_expect(wave < 11, 1)) {
      const int qt = bx < 72 ? (bx * 57) >> 10 : ((bx - 72) * 57) >> 11, ln = bx < 72 ? bx - 18 * qt : bx - 54 - 36 * qt;
      const int j = wave - 8, r0 = 144 * qt + 48 * j + 6 * (lane >> 3);
      const rsrc_t rc = mkrs(cv, D * 2), rm = mkrs(wmod, bx < 216 ? D * 6 * D * 2 : 0);
      const int mo = r0 * (6 * D * 2) + (64 * ln + 8 * (lane & 7)) * 2;
      u4 wm[6];
#pragma unroll
      for (int i = 0; i < 6; ++i) wm[i] = __builtin_amdgcn_raw_buffer_load_b128(rm, mo, i * (6 * D * 2), NT);
      const u3 cr = __builtin_amdgcn_raw_buffer_load_b96(rc, r0 * 2, 0, 0);
      const u32 ep = __builtin_amdgcn_raw_buffer_load_b32(rws, O_EPOCH, 0, SC1);
      SB();
      STAMPK(11);
      float a[8];
#pragma unroll
      for (int e = 0; e < 8; ++e) a[e] = 0.f;
      u32 P[3][8];
#pragma unroll
      for (int p = 0; p < 3; ++p)
#pragma unroll
        for (int e = 0; e < 4; ++e) { const u32 u = wm[2 * p][e], v = wm[2 * p + 1][e]; P[p][2 * e] = plo(u, v); P[p][2 * e + 1] = phi(u, v); }
#pragma unroll
      for (int p = 0; p < 3; ++p) {
        const u32 w = cr[p], act = pk(silu(lo16(w)), silu(hi16(w)));
#pragma unroll
        for (int e = 0; e < 8; ++e) a[e] = dot2(P[p][e], act, a[e]);
      }
      float t[4], u[2];
#pragma unroll
      for (int i = 0; i < 4; ++i) t[i] = sw32(a[i], a[i + 4]);
#pragma unroll
      for (int i = 0; i < 2; ++i) u[i] = sw16(t[i], t[i + 2]);
      const bool b3 = lane & 8;
      sh->mp[j][8 * (lane & 7) + 2 * (lane >> 4) + ((lane >> 3) & 1)] = (b3 ? u[1] : u[0]) + dpp<ROR8>(b3 ? u[0] : u[1]);
      lds_set(&sh->flag[j], lane);
      const u32 tag = __builtin_amdgcn_readfirstlane(((ep | 0x40004000u) + 1) & 0xbfffbfffu);
      STAMP(1);
      if (wave == 8 && bx < 216) {
        lds_wait<1>(&sh->flag[1]); lds_wait<1>(&sh->flag[2]);
        const int l = 2 * (lane & 31);
        const f2 m = *(const LAS f2*)&sh->mp[0][l] + *(const LAS f2*)&sh->mp[1][l] + *(const LAS f2*)&sh->mp[2][l];
        const float m0 = m[0], m1 = m[1];
        const u4 o = {asu(m0), tag, asu(m1), tag};
        if (lane < 32) st128(o, rws, O_MOD + qt * MOD_STR + ln * 512 + lane * 16);
        STAMP(2);
      }
      if (b < 96) {
        const int hd = b & 31, cl = 3 * (b >> 5) + j;
        const rsrc_t rwo = mkrs(wo + (size_t)hd * 128 * D, X_NOWO ? 0 : 128 * D * 2);
        const int o = (16 * (lane >> 4) + (lane & 8)) * (D * 2) + 128 * cl + 16 * (lane & 7);
        u4 w[16], F[16];
#pragma unroll
        for (int i = 0; i < 16; ++i) { if (i >= 2 * HW) VMCNT(2 * HW - 1); w[i] = __builtin_amdgcn_raw_buffer_load_b128(rwo, o, (64 * (i >> 3) + (i & 7)) * (D * 2), NT); SB(); }
        frag<4>(w, F);
        frag<4>(w + 8, F + 8);
        int yo = O_YA + ((2 * cl + ((lane & 7) >> 2)) * 32 + hd) * 128 + (2 * (lane & 3) + (lane >> 5)) * 16;
        int po = O_OP + (8 * hd + ((lane & 15) >> 1)) * 512 + (2 * (lane >> 4) + (lane & 1)) * 16;
        Land ps;
        u32 v[8] = {};
        PIN(yo); PIN(po);
        STAMP(5);
        bar();
        STAMP(8);
        if (ODL) __builtin_amdgcn_s_sleep(ODL);
        ppoll4<DPO, GPO, 128, 32 * OTH, YSL>(ps, v, rs, po, tag);
        STAMPT(10);
        const u4 a0 = cat(v), a1 = cat(v + 4);
        float y[8], t4[4];
#pragma unroll
        for (int i = 0; i < 8; ++i) {
          const f4 z = {0.f, 0.f, 0.f, 0.f};
          const f4 d = mfma(a1, F[8 + i], mfma(a0, F[i], z));
          const f2 e = d.xy + d.zw;
          const float e0 = e[0], e1 = e[1];
          y[i] = e0 + dpp<ROR8>(e1);
        }
#pragma unroll
        for (int i = 0; i < 4; ++i) t4[i] = sw32(y[i], y[i + 4]);
        u32 r0, r1;
        swap16(pk(sw16(t4[0], t4[2]), sw16(t4[1], t4[3])), r0, r1);
        if ((lane & 24) == 0) st128(u4{r0, tag, r1, tag}, rws, yo);
        STAMPT(11);
        drain(ps);
      } else if (b >= S2) {
        const int f = b - S2;
        const rsrc_t rwd = mkrs(wd + (size_t)(128 * (f >> 2) + 2 * (f & 3)) * D, X_NOWO ? 0 : 122 * D * 2);
        const int o = 32 * (lane >> 4) * (D * 2) + (192 * j + 4 * (lane & 15)) * 2;
        u2 w[3][8];
        u4 F[12];
#pragma unroll
        for (int i = 0; i < 8; ++i) {
          if (i >= WDW) VMCNT(3 * WDW - 3);
#pragma unroll
          for (int e = 0; e < 3; ++e) w[e][i] = __builtin_amdgcn_raw_buffer_load_b64(rwd, o + e * 128, (8 * (i >> 1) + (i & 1)) * (D * 2), NT);
          SB();
        }
#pragma unroll
        for (int e = 0; e < 3; ++e) frag<2>(w[e], F + 4 * e);
        int so = O_S + (f * 18 + 6 * j) * 512 + lane * 16, to = O_ST + f * STF + lane * STR_;
        int fo = O_FC + ((6 * j + ((lane & 15) >> 3)) * 144 + f) * 128 + (lane & 7) * 16;
        LAS float* gw = &sh->mp[j][32 * ((lane >> 4) & 1) + 2 * (lane & 15) + (lane >> 5)];
        const LAS float* gr = &sh->mp[0][32 * ((lane >> 3) & 1) + 8 * (lane >> 4) + (lane & 7)];
        Land ps;
        u32 v[8] = {};
        PIN(so); PIN(to); PIN(fo); PIN(gw); PIN(gr);
        STAMP(5);
        bar(); bar();
        STAMP(8);
        bar();
        STAMPT(10);
        ppoll7<SDL, GPS, O_AB - O_S>(ps, v, rs, so, to, (1u << 18) - 1, tag);
        STAMPT(11);
        float e[3][2];
#pragma unroll
        for (int i = 0; i < 2; ++i) {
          const u32 s0 = v[i], s1 = v[2 + i], s2 = v[4 + i], c0 = ps.c[2 * i], c1 = ps.c[4 + 2 * i], c2 = ps.c[8 + 2 * i];
          e[0][i] = (asf(s0) + asf(s1)) + asf(s2);
          e[1][i] = (lo16(c0) + lo16(c1)) + lo16(c2);
          e[2][i] = (hi16(c0) + hi16(c1)) + hi16(c2);
        }
        const float ea = sw32(e[1][0], e[1][1]), eb = sw32(e[2][0], e[2][1]);
        const u32 t0 = v[6], t1 = v[7];
        float st = row_sum(sw16(asf(t0), asf(t1)));
        const float mean = asf(__builtin_amdgcn_readlane(asu(st), 0)) * (1.f / D), e2 = asf(__builtin_amdgcn_readlane(asu(st), 16)) * (1.f / D);
        const float rsd = __builtin_amdgcn_rsqf(fmaxf(fmaf(-mean, mean, e2), 0.f) + 1e-5f);
        *gw = fmaf(fmaf(-mean, ea, sw32(e[0][0], e[0][1])), rsd, eb);
        bar();
        STAMPT(12);
        const float gs = (gr[0] + gr[64]) + gr[128];
        const float us = dpp<SHL + 8>(gs);
        const u4 a4 = gather8(silu(gs) * us);
#pragma unroll
        for (int k = 0; k < 3; ++k) {
          float r[4];
#pragma unroll
          for (int i = 0; i < 4; ++i) { f4 z = {0.f, 0.f, 0.f, 0.f}; const f4 t4 = mfma(a4, F[4 * k + i], z); r[i] = t4[0]; }
          const u4 o4 = {pk(r[0], r[1]), tag, pk(r[2], r[3]), tag};
          if (lane < 16) st128(o4, rws, fo + k * 2 * 144 * 128);
        }
        STAMPT(13);
        drain(ps);
      } else if (wave == 8) {
        const rsrc_t rg = gu_rs(wg, wu, 0, rb1, b & 7, true);
        const int go = 8 * (lane >> 4) * (FF * 2) + (lane & 15) * 16;
        u4 G[8];
#pragma unroll
        for (int i = 0; i < 8; ++i) { if (i >= 2) VMCNT(1); G[i] = __builtin_amdgcn_raw_buffer_load_b128(rg, go, i * FF * 2, NT); SB(); }
#pragma unroll
        for (int i = 0; i < 8; ++i) PIN(G[i]);
        bar(); bar();
        GU_TAIL(0, b & 7)
      }
    } else {
      const rsrc_t rx = mkrs(x, D * 2), rq = mkrs(wq, D * 4096 * 2);
      const int g = lane >> 4, pc = (lane & 15) < 9 ? (lane & 15) : 8, pidx = 36 * s + 9 * g + pc;
      const u4 xa = __builtin_amdgcn_raw_buffer_load_b128(rx, lane * 16, 0, 0);
      const u4 xb = __builtin_amdgcn_raw_buffer_load_b128(rx, 1024 + lane * 16, 0, 0);
      const u32 xp = __builtin_amdgcn_raw_buffer_load_b32(rx, pidx * 4, 0, 0);
      const u32 ep = __builtin_amdgcn_raw_buffer_load_b32(rws, O_EPOCH, 0, SC1);
      u4 wv[18];
      const int qo = (72 * s + 18 * g) * 8192 + (128 * h + 8 * (lane & 15)) * 2;
#pragma unroll
      for (int i = 0; i < 18; ++i) { if (i >= WQW) VMCNT(WQW - 1); wv[i] = __builtin_amdgcn_raw_buffer_load_b128(rq, qo, i * 8192, NT); SB(); }
      STAMPK(11);
      const u32 tag = __builtin_amdgcn_readfirstlane(((ep | 0x40004000u) + 1) & 0xbfffbfffu);
      sh->tag = tag;
      float l0, l1;
      {
        float sum = 0.f;
#pragma unroll
        for (int i = 0; i < 4; ++i) { const u32 u = xa[i], v = xb[i]; sum += (lo16(u) + hi16(u)) + (lo16(v) + hi16(v)); }
        const float mean = wave_sum(sum) * (1.f / D);
        float sa = 0.f, sb = 0.f;
#pragma unroll
        for (int i = 0; i < 4; ++i) {
          const u32 u = xa[i], v = xb[i];
          const float d0 = lo16(u) - mean, d1 = hi16(u) - mean, d2 = lo16(v) - mean, d3 = hi16(v) - mean;
          sa = fmaf(d0, d0, fmaf(d1, d1, sa)); sb = fmaf(d2, d2, fmaf(d3, d3, sb));
        }
        const float rs = ln_rstd(wave_sum(lane < 8 ? sa + sb : sa) * (1.f / D));
        l0 = rb(rb(lo16(xp) - rb(mean)) * rs); l1 = rb(rb(hi16(xp) - rb(mean)) * rs);
        PIN(l0); PIN(l1);
      }
      u32 P[9][8];
#pragma unroll
      for (int p = 0; p < 9; ++p)
#pragma unroll
        for (int e = 0; e < 4; ++e) { const u32 u = wv[2 * p][e], v = wv[2 * p + 1][e]; P[p][2 * e] = plo(u, v); P[p][2 * e + 1] = phi(u, v); PIN(P[p][2 * e]); PIN(P[p][2 * e + 1]); }
      STAMP(1);
      {
        int o = (X_NOPOLL & 1) ? O_END : O_MOD + pidx * 16;
        u4 v[8];
        if (PRIO) __builtin_amdgcn_s_setprio(PRIO);
        until(tz, TP1);
#pragma nounroll
        for (u32 n = 0; n < SPIN_G; ++n) {
          FENCE(); PIN(o);
          u32 bad = 0;
#pragma unroll
          for (int i = 0; i < 8; ++i) v[i] = ld128(rws, o + (i >> 1) * MOD_STR + (i & 1) * 4608);
#pragma unroll
          for (int i = 0; i < 8; ++i) bad |= (v[i][1] ^ tag) | (v[i][3] ^ tag);
          if (__all(bad == 0) || (X_NOPOLL & 1)) break;
          __builtin_amdgcn_s_sleep(PSL);
        }
        STAMP(2);
        float m[4];
#pragma unroll
        for (int i = 0; i < 4; ++i) {
          const u32 e0 = v[i >> 1][2 * (i & 1)], e1 = v[2 + (i >> 1)][2 * (i & 1)], e2 = v[4 + (i >> 1)][2 * (i & 1)], e3 = v[6 + (i >> 1)][2 * (i & 1)];
          m[i] = rb((asf(e0) + asf(e1)) + (asf(e2) + asf(e3)));
        }
        const u32 hp = pk(rb(l0 * rb(1.f + m[2])) + m[0], rb(l1 * rb(1.f + m[3])) + m[1]);
        float a[8];
#pragma unroll
        for (int e = 0; e < 8; ++e) a[e] = 0.f;
#pragma unroll
        for (int p = 0; p < 9; ++p) {
          const u32 hb = nbc(hp, p);
#pragma unroll
          for (int e = 0; e < 8; ++e) a[e] = dot2(P[p][e], hb, a[e]);
        }
        float t[4];
#pragma unroll
        for (int i = 0; i < 4; ++i) t[i] = sw32(a[i], a[i + 4]);
        const u4 qo4 = {asu(sw16(t[0], t[2])), tag, asu(sw16(t[1], t[3])), tag};
        st128(qo4, rws, O_QP + pi * 1024 + lane * 16);
      }
      {
        int o = (X_NOPOLL & 2) ? O_END : O_QP + h * 8192 + lane * 16;
        u4 v[8];
        until(tz, TP2);
#pragma nounroll
        for (u32 n = 0; n < SPIN_G; ++n) {
          FENCE(); PIN(o);
          u32 bad = 0;
#pragma unroll
          for (int i = 0; i < 8; ++i) v[i] = ld128(rws, o + i * 1024);
#pragma unroll
          for (int i = 0; i < 8; ++i) bad |= (v[i][1] ^ tag) | (v[i][3] ^ tag);
          if (__all(bad == 0) || (X_NOPOLL & 2)) break;
          __builtin_amdgcn_s_sleep(PSL);
        }
        float q0 = 0.f, q1 = 0.f;
#pragma unroll
        for (int i = 0; i < 8; ++i) { const u32 u = v[i][0], w = v[i][2]; q0 += asf(u); q1 += asf(w); }
        sh->q[4 * (lane & 15) + g] = pk(q0, q1);
        lds_set(&sh->flag[3], lane);
      }
      STAMP(3);
      if (b >= S1 || b < 18) {
        const int pr = 16 * rb1 + (lane & 15);
        const u32 xq = __builtin_amdgcn_raw_buffer_load_b32(rx, pr * 4, 0, 0);
        u4 v[4];
        poll<4, MOD_STR, PSL>(v, rs, O_MOD + (2 + g) * 4608 + pr * 16, tag);
        float m[2];
#pragma unroll
        for (int i = 0; i < 2; ++i) {
          const u32 e0 = v[0][2 * i], e1 = v[1][2 * i], e2 = v[2][2 * i], e3 = v[3][2 * i];
          m[i] = rb((asf(e0) + asf(e1)) + (asf(e2) + asf(e3))) + (g == 2 ? 1.f : 0.f);
        }
        sh->am[(0x5234 >> (4 * g)) & 7][lane & 15] = pk(m[0], m[1]);
        sh->am[6][lane & 15] = xq;
      }
      float M;
      {
#pragma nounroll
        for (u32 n = 0; n < SPIN_L; ++n) {
          if (__all(((volatile LAS u32*)sh->flag)[8 + (lane & 7)] != 0)) break;
          __builtin_amdgcn_s_sleep(4);
        }
        FENCE();
        const f2 m = sh->ml[lane & 7];
        M = max8(m[0]);
        const float L = sum8(m[1] * __expf(m[0] - M));
        const u4 o = {asu(M), tag, asu(L), tag};
        if (lane == 0) st128(o, rws, O_ML + pi * 16);
        sh->gm = M;
        lds_set(&sh->flag[4], lane);
      }
      STAMP(4);
      lds_wait<8>(&sh->flag[6]);
      float cw;
      {
        u4 m;
        poll<1, 0, PSL>(&m, rs, O_ML + (8 * h + (lane & 7)) * 16, tag);
        const u32 mu = m[0], lu = m[2];
        const float Mh = max8(asf(mu));
        cw = __expf(M - Mh) / sum8(asf(lu) * __expf(asf(mu) - Mh));
      }
      STAMP(6);
      {
        const LAS f2* p = &sh->ob[0][((lane >> 1) & 7) + 8 * (lane >> 5) + 16 * (lane & 1) + 40 * ((lane >> 4) & 1)];
        int oo = O_OP + pi * 512 + lane * 8;
        PIN(p); PIN(oo); PIN(cw);
        STAMP(7);
        bar();
        STAMP(8);
        f2 t[8];
#pragma unroll
        for (int i = 0; i < 8; ++i) t[i] = p[72 * i];
        const f2 r = (((t[0] + t[1]) + (t[2] + t[3])) + ((t[4] + t[5]) + (t[6] + t[7]))) * cw;
        const float r0 = r[0], r1 = r[1];
        const u2 o = {pk(r0, r1), tag};
        __builtin_amdgcn_raw_buffer_store_b64(o, rws, oo, 0, STM);
      }
      if (b >= S1 || b < 18) {
        if (b >= S1) bar();
        STAMP(9);
        float ga[4], xi[4], c1[4];
        {
          const u2 a = *(const LAS u2*)&sh->am[4][2 * (lane & 7)], e = *(const LAS u2*)&sh->am[6][2 * (lane & 7)], c = *(const LAS u2*)&sh->am[2][2 * (lane & 7)];
#pragma unroll
          for (int i = 0; i < 2; ++i) {
            const u32 a_ = a[i], e_ = e[i], c_ = c[i];
            ga[2 * i] = lo16(a_); ga[2 * i + 1] = hi16(a_); xi[2 * i] = lo16(e_); xi[2 * i + 1] = hi16(e_); c1[2 * i] = lo16(c_); c1[2 * i + 1] = hi16(c_);
          }
#pragma unroll
          for (int i = 0; i < 4; ++i) { PIN(ga[i]); PIN(xi[i]); PIN(c1[i]); }
        }
        u4 sel = selector(lane);
        u32 msk = (lane & 8) ? 0xffff0000u : 0u;
        LAS u2* aw = (LAS u2*)&sh->am[(lane >> 3) & 1][2 * (lane & 7)];
        PIN(sel); PIN(msk); PIN(aw);
        Land ps;
        u32 v[8] = {};
        int yo = O_YA + rb1 * 4096 + lane * 16;
        int to = b >= S1 && lane < (b < S2 ? 18 : 16) ? O_ST + (lane < 16 ? 16 * cb1 + lane : 2 * cb1 + lane - 18) * STF + rb1 * STR_ : O_END;
        PIN(yo); PIN(to);
        if (YDL) __builtin_amdgcn_s_sleep(YDL);
        ppoll4<DPY, GPY, 1024, 8 * ATH, ASL>(ps, v, rs, yo, tag);
        STAMPT(10);
        const f4 z4 = {0.f, 0.f, 0.f, 0.f};
        const f4 y = fold8(mfma(sel, cat(v), z4) + mfma(sel, cat(v + 4), z4));
        const u32 d0 = pk(fmaf(ga[0], y[0], xi[0]), fmaf(ga[1], y[1], xi[1])), d1 = pk(fmaf(ga[2], y[2], xi[2]), fmaf(ga[3], y[3], xi[3]));
        const float x2[4] = {lo16(d0), hi16(d0), lo16(d1), hi16(d1)};
        u32 z[4];
#pragma unroll
        for (int i = 0; i < 4; ++i) { const float t = x2[i] * c1[i]; z[i] = asu(t - asf(asu(t) & msk)); }
        *aw = u2{phi(z[0], z[1]), phi(z[2], z[3])};
        SB();
        const float s1 = sum8((x2[0] + x2[1]) + (x2[2] + x2[3])), s2 = sum8(fmaf(x2[0], x2[0], x2[1] * x2[1]) + fmaf(x2[2], x2[2], x2[3] * x2[3]));
        st128(u4{asu(s1), tag, asu(s2), tag}, rws, to);
        bar();
        STAMPT(11);
        if (b < S1) {
          if (b == 0 && lane == 0) __builtin_amdgcn_raw_buffer_store_b32(tag, rws, O_EPOCH, 0, SC1);
          const u2 gb = *(const LAS u2*)&sh->am[5][2 * (lane & 7)];
          const u32 gb0 = gb[0], gb1 = gb[1];
          drain(ps);
          Land fs;
          const LAS f4* p = (const LAS f4*)&sh->ob[0][0] + 4 * (lane & 8) + (lane & 7);
          PIN(p);
          const f4 d = last_sum(fs, rs, rb1, 8, lane, tag);
          STAMPT(12);
          bar();
          const f4 r = fold8(((p[0] + p[8]) + (p[16] + p[24])) + d);
          const u2 o = {pk(fmaf(lo16(gb0), r[0], x2[0]), fmaf(hi16(gb0), r[1], x2[1])), pk(fmaf(lo16(gb1), r[2], x2[2]), fmaf(hi16(gb1), r[3], x2[3]))};
          __builtin_amdgcn_raw_buffer_store_b64(o, mkrs(out + 32 * rb1, 64), lane * 8, 0, 0);
          STAMPT(13);
          drain(fs);
        } else {
          drain(ps);
        }
      }
    }
    if (CPF) asm volatile("" ::"v"(cpf));
#pragma unroll
    for (int t = 0; t < WK; ++t) asm volatile("" ::"v"(kb[t][0]));
  } else {
    const rsrc_t rv = mkrs(vc + slab, 262144);
    u4 vb[32], qv;
    float sp[32], s0 = 0.f, mw = 0.f, p0 = 0.f, p1 = 0.f, acc[8];
#pragma unroll
    for (int i = 0; i < 8; ++i) acc[i] = 0.f;
#define QK(i) do { float a_ = dot2(kb[i][0], qv[0], 0.f), b_ = dot2(kb[i][1], qv[1], 0.f); a_ = dot2(kb[i][2], qv[2], a_); b_ = dot2(kb[i][3], qv[3], b_); sp[i] = a_ + b_; PIN(sp[i]); } while (0)
#define PV(i) do { const u32 pb_ = nbc(asu((i) < 16 ? p0 : p1), (i) & 15), pl_ = pb_ >> 16; \
      _Pragma("unroll") for (int cc = 0; cc < 4; ++cc) { const u32 w_ = vb[i][cc]; acc[2 * cc] = dot2(w_, pl_, acc[2 * cc]); acc[2 * cc + 1] = dot2(w_, pb_, acc[2 * cc + 1]); } \
      _Pragma("unroll") for (int cc = 0; cc < 8; ++cc) PIN(acc[cc]); } while (0)
    sfor<WK, 64>([&](auto T) __attribute__((always_inline)) {
      constexpr int t = T.value;
      if constexpr (PS > 0 && t >= P0) {
        asm volatile("s_memrealtime vcc\n\ts_waitcnt vmcnt(%1) lgkmcnt(0)\n\ts_sub_u32 vcc_lo, vcc_lo, %0\n\ts_cmpk_ge_u32 vcc_lo, %2\n\ts_cbranch_scc1 1f\n\ts_waitcnt vmcnt(%3)\n\t"
                     ".if %4>0\n\ts_cmpk_ge_u32 vcc_lo, %4\n\ts_cbranch_scc1 1f\n\ts_waitcnt vmcnt(%5)\n\t.endif\n1:"
                     :: "s"(tz), "n"(WV - 1), "n"(due(t)), "n"(WK - 1), "n"(PE ? due(t) - PE : 0), "n"(WK - 2) : "memory", "vcc", "scc");
      } else {
        VMCNT(win(t) - 1);
      }
      if constexpr (t == WK) { if (CPF) asm volatile("" ::"v"(cpf)); STAMP(1); }
      if constexpr (t == 32) STAMP(4);
      if constexpr (t < 32) kb[t] = __builtin_amdgcn_raw_buffer_load_b128(rk, vo, t * 4096, NT);
      else vb[t - 32] = __builtin_amdgcn_raw_buffer_load_b128(rv, vo, (t - 32) * 4096, NT);
      SB();
      if constexpr (t == QS) {
        STAMP(2);
        lds_wait<1>(&sh->flag[3]);
        STAMP(3);
        qv = *(const LAS u4*)(sh->q + 4 * (lane & 15));
      }
      if constexpr (t >= QS && t < QS + 4) {
#pragma unroll
        for (int j = 8 * (t - QS); j < 8 * (t - QS) + 8; ++j) QK(j);
        if constexpr (t == QS + 1) { s0 = tred16(sp, lane); PIN(s0); }
      }
      if constexpr (t == QS + 4) {
        float s1 = tred16(sp + 16, lane);
        s0 = rb(rb(s0) * 0.08838834764831845f);
        s1 = rb(rb(s1) * 0.08838834764831845f);
        mw = wave_max(fmaxf(s0, s1));
        p0 = rb(__expf(s0 - mw)); p1 = rb(__expf(s1 - mw));
        const float lw = wave_sum(p0 + p1);
        const f2 w = {mw, lw};
        sh->ml[wave] = w;
        LDSW();
        *(volatile LAS u32*)&sh->flag[8 + wave] = 1;
        STAMP(5);
      }
      if constexpr (t > QS + 4) {
        constexpr int j0 = pv_done(t - 1), j1 = pv_done(t);
#pragma unroll
        for (int j = j0; j < j1; ++j) PV(j);
      }
      if constexpr (t == LATE) *(volatile LAS u32*)&sh->flag[6] = 1;
      if constexpr (t == 48) STAMP(6);
      SB();
    });
    const rsrc_t rg = gu_rs(wg, wu, cb1, rb1, wave, b >= S1);
    const int go = 8 * (lane >> 4) * (FF * 2) + (lane & 15) * 16;
    u4 G[8];
    sfor<0, GN>([&](auto I) __attribute__((always_inline)) { G[I.value] = __builtin_amdgcn_raw_buffer_load_b128(rg, go, I.value * FF * 2, NT); });
    SB();
    lds_wait<1>(&sh->flag[4]);
    float fw = __expf(mw - sh->gm);
    const u32 tag = __builtin_amdgcn_readfirstlane(sh->tag);
    PIN(fw);
    constexpr int PD = pv_done(63);
#pragma unroll
    for (int j = PD; j < 32; ++j) { PV(j); SB(); }
    float t4[4];
#pragma unroll
    for (int i = 0; i < 4; ++i) t4[i] = sw32(acc[i], acc[i + 4]);
    const f2 o2 = {sw16(t4[0], t4[2]) * fw, sw16(t4[1], t4[3]) * fw};
    sh->ob[wave][lane + 8 * (lane >> 5)] = o2;
    STAMP(7);
    bar();
    STAMP(8);
    if (b >= S1) {
      bar();
      sfor<GN, 8>([&](auto I) __attribute__((always_inline)) { G[I.value] = __builtin_amdgcn_raw_buffer_load_b128(rg, go, I.value * FF * 2, NT); });
      SB();
      GU_TAIL(cb1, wave)
    } else {
#pragma unroll
      for (int i = 0; i < GN; ++i) asm volatile("" ::"v"(G[i][0]));
      if (b < 18) {
        Land fs;
        LAS f4* p = (LAS f4*)&sh->ob[0][0] + 8 * wave + (lane & 7);
        PIN(p);
        bar();
        *p = fold8(last_sum(fs, rs, rb1, wave, lane, tag));
        STAMPT(14);
        bar();
        drain(fs);
      }
    }
  }
  STAMP(15);
  STAMP_OUT();
}

extern "C" void kernel_launch(void* const* d_in, const int* in_sizes, int n_in, void* d_out, int out_size, void* d_ws, size_t ws_size,
                              hipStream_t stream) {
  fused_kernel<<<256, 768, 0, stream>>>((const u16*)d_in[0], (const u16*)d_in[1], (const u16*)d_in[2], (const u16*)d_in[3],
                                        (const u16*)d_in[4], (const u16*)d_in[6], (const u16*)d_in[7], (const u16*)d_in[8],
                                        (const u16*)d_in[9], (const u16*)d_in[10], (u16*)d_out, (char*)d_ws + WSO(ws_size));
}
```

```cpp
#include <hip/hip_runtime.h>
#include <stdint.h>

#define STAMPS 0
#define PS 1030
#define P0 8
#define POFF 80
#define PKNOTS 90, 190, 350, 605, 900, 1145, 1415, 1650, 1880, 2105
#define YSL 12
#define FDL 36
#define DPO 1
#define DPY 3
#define DPF 3
#define GPY 5
#define GPF 5
#define OTH 3
#define WDW 1
#define YR 16
#define YDL 40
#define ASL 2
#define ATH 7
#define CW 8
#define DPS 3
#define GPS 5
#define SDL 0
#define STB 1
#define GPO 5
#define HQ 0
#define RO 72
#define WK 3
#define WV 5
#define PV0 4
#define GN 2
#define LATE 54
#define CPF 24100
#define PSL 16
#define TP1 700
#define TP2 1000
#define HW 2

typedef unsigned u32;
typedef unsigned long long u64;
typedef unsigned short u16;
typedef u32 u1 __attribute__((ext_vector_type(1)));
typedef u32 u2 __attribute__((ext_vector_type(2)));
typedef u32 u3 __attribute__((ext_vector_type(3)));
typedef u32 u4 __attribute__((ext_vector_type(4)));
typedef float f2 __attribute__((ext_vector_type(2)));
typedef float f4 __attribute__((ext_vector_type(4)));
typedef __bf16 bf2 __attribute__((ext_vector_type(2)));
typedef __bf16 bfx8 __attribute__((ext_vector_type(8)));
typedef __amdgpu_buffer_rsrc_t rsrc_t;
#define LAS __attribute__((address_space(3)))
#define GAS __attribute__((address_space(1)))
#define DI __device__ __forceinline__

constexpr int D = 576, FF = 4608;
constexpr int SC1 = 16, NT = 2;
constexpr u32 SPIN_G = 1u << 17, SPIN_L = 1u << 20;

#define V10
#define V12
constexpr int O_EPOCH = 0;
constexpr int O_MC = 2048;
constexpr int O_ML = 12288;
constexpr int O_MOD = 16384, MOD_STR = 3456 * 8;
constexpr int O_QP = 131072;
constexpr int O_OP = O_QP + 262144;
constexpr int O_FC = O_OP + 131072;
constexpr int O_YA = O_FC + 18 * 144 * 128;
constexpr int O_S = O_YA + YR * 9 * 2048, S_STR = 9 * 512 + 256;
constexpr int O_AB = O_S + 144 * S_STR;
constexpr int O_END = O_AB + 144 * S_STR;
constexpr int O_STAMP = 4 << 20;
constexpr int NY = 72, FE = NY + 18, S1 = 94, S2 = 112;

DI float asf(u32 x) { return __uint_as_float(x); }
DI u32 asu(float x) { return __float_as_uint(x); }
DI u32 pk(float lo, float hi) { f2 t = {lo, hi}; bf2 r = __builtin_convertvector(t, bf2); return __builtin_bit_cast(u32, r); }
DI float rb(float x) { return asf(pk(0.f, x)); }
DI float lo16(u32 x) { return asf(x << 16); }
DI float hi16(u32 x) { return asf(x & 0xffff0000u); }
template <int C> DI u32 dppu(u32 x) { return (u32)__builtin_amdgcn_update_dpp(0, (int)x, C, 0xf, 0xf, true); }
template <int C> DI float dpp(float x) { return asf(dppu<C>(asu(x))); }
constexpr int ROR4 = 0x124, ROR8 = 0x128, HMIR = 0x141, QX1 = 0xB1, QX2 = 0x4E, SHL = 0x100, NBC = 0x150;
DI u32 nbc(u32 src, int n) {
  switch (n) {
#define CASE(n) case n: return dppu<NBC + n>(src);
    CASE(0) CASE(1) CASE(2) CASE(3) CASE(4) CASE(5) CASE(6) CASE(7) CASE(8) CASE(9) CASE(10) CASE(11) CASE(12) CASE(13) CASE(14)
#undef CASE
    default: return dppu<NBC + 15>(src);
  }
}
DI float dot2(u32 a, u32 b, float c) { return __builtin_amdgcn_fdot2_f32_bf16(__builtin_bit_cast(bf2, a), __builtin_bit_cast(bf2, b), c, false); }
DI float sw32(float a, float b) { auto r = __builtin_amdgcn_permlane32_swap(asu(a), asu(b), false, false); u32 r0 = r[0], r1 = r[1]; return asf(r0) + asf(r1); }
DI float sw16(float a, float b) { auto r = __builtin_amdgcn_permlane16_swap(asu(a), asu(b), false, false); u32 r0 = r[0], r1 = r[1]; return asf(r0) + asf(r1); }
DI float mx32(float a) { auto r = __builtin_amdgcn_permlane32_swap(asu(a), asu(a), false, false); u32 r0 = r[0], r1 = r[1]; return fmaxf(asf(r0), asf(r1)); }
DI float mx16(float a) { auto r = __builtin_amdgcn_permlane16_swap(asu(a), asu(a), false, false); u32 r0 = r[0], r1 = r[1]; return fmaxf(asf(r0), asf(r1)); }
DI float row_sum(float v) { v += dpp<QX1>(v); v += dpp<QX2>(v); v += dpp<ROR4>(v); return v + dpp<ROR8>(v); }
DI float wave_sum(float v) { v = row_sum(v); v = sw16(v, v); return sw32(v, v); }
DI float wave_max(float v) {
  v = fmaxf(v, dpp<QX1>(v)); v = fmaxf(v, dpp<QX2>(v)); v = fmaxf(v, dpp<ROR4>(v)); v = fmaxf(v, dpp<ROR8>(v));
  return mx32(mx16(v));
}
DI float sum8(float v) { v += dpp<QX1>(v); v += dpp<QX2>(v); return v + dpp<HMIR>(v); }
DI float max8(float v) { v = fmaxf(v, dpp<QX1>(v)); v = fmaxf(v, dpp<QX2>(v)); return fmaxf(v, dpp<HMIR>(v)); }
DI u32 plo(u32 a, u32 b) { return __builtin_amdgcn_perm(b, a, 0x05040100u); }
DI u32 phi(u32 a, u32 b) { return __builtin_amdgcn_perm(b, a, 0x07060302u); }
DI f4 mfma(u4 a, u4 b, f4 c) { return __builtin_amdgcn_mfma_f32_16x16x32_bf16(__builtin_bit_cast(bfx8, a), __builtin_bit_cast(bfx8, b), c, 0, 0, 0); }
DI rsrc_t mkrs(const void* p, u32 n) { return __builtin_amdgcn_make_buffer_rsrc((void*)p, 0, n, 0x00020000); }
#define FENCE() asm volatile("" ::: "memory")
#define SB() __builtin_amdgcn_sched_barrier(0)
#define PIN(v) asm volatile("" : "+v"(v))
#define VMCNT(n) asm volatile("s_waitcnt vmcnt(%0)" ::"n"(n) : "memory")
#define LDSW() asm volatile("s_waitcnt lgkmcnt(0)" ::: "memory")
DI void bar() { asm volatile("s_waitcnt lgkmcnt(0)\n\ts_barrier" ::: "memory"); }
DI float silu(float v) { return v * __builtin_amdgcn_rcpf(1.f + __expf(-v)); }
DI u4 ld128(rsrc_t r, int o) { return __builtin_amdgcn_raw_buffer_load_b128(r, o, 0, SC1); }
DI u2 ld64(rsrc_t r, int o) { return __builtin_amdgcn_raw_buffer_load_b64(r, o, 0, SC1); }
DI void st128(u4 v, rsrc_t r, int o) { __builtin_amdgcn_raw_buffer_store_b128(v, r, o, 0, SC1); }
DI float ln_rstd(float var) { return __builtin_amdgcn_rcpf(rb(__builtin_amdgcn_sqrtf(rb(rb(var) + 1e-5f)))); }

struct __attribute__((aligned(16))) Sh {
  f2 ob[8][72];
  float mp[4][64];
  float un[6][64];
  u4 zz[2][64];
  u32 mc[4][32];
  u32 q[64];
  f2 ml[8];
  float gm;
  u32 tag;
  u32 pad[2];
  u32 flag[16];
};

#define STAMP(i)
#define STAMP_OUT()

template <int S> DI void lds_wait(volatile LAS u32* f) {
#pragma nounroll
  for (u32 n = 0; n < SPIN_L; ++n) { if (*f != 0) break; __builtin_amdgcn_s_sleep(S); }
  FENCE();
}
DI void until(u32 tz, u32 t) {
#pragma nounroll
  for (u32 n = 0; n < 4096; ++n) { if ((u32)__builtin_amdgcn_s_memrealtime() - tz >= t) break; __builtin_amdgcn_s_sleep(2); }
}
DI void lds_set(LAS u32* f, int lane) { LDSW(); if (lane == 0) *(volatile LAS u32*)f = 1; }

DI float tred16(const float* s, int lane) {
  float t[8], u[4], v[2];
  const bool b3 = lane & 8, b2 = lane & 4, b1 = lane & 2, b0 = lane & 1;
#pragma unroll
  for (int i = 0; i < 8; ++i) t[i] = (b3 ? s[i + 8] : s[i]) + dpp<ROR8>(b3 ? s[i] : s[i + 8]);
#pragma unroll
  for (int i = 0; i < 4; ++i) u[i] = (b2 ? t[i + 4] : t[i]) + dpp<HMIR>(b2 ? t[i] : t[i + 4]);
#pragma unroll
  for (int i = 0; i < 2; ++i) v[i] = (b1 ? u[i + 2] : u[i]) + dpp<QX2>(b1 ? u[i] : u[i + 2]);
  return (b0 ? v[1] : v[0]) + dpp<QX1>(b0 ? v[0] : v[1]);
}

template <int C, typename V> DI void frag(const V* L, u4* F) {
#pragma unroll
  for (int c = 0; c < C; ++c) {
    u4 a, b;
#pragma unroll
    for (int j = 0; j < 4; ++j) { const u32 x = L[2 * j][c], y = L[2 * j + 1][c]; a[j] = plo(x, y); b[j] = phi(x, y); }
    PIN(a); PIN(b);
    F[2 * c] = a; F[2 * c + 1] = b;
  }
}

DI u4 gather8(float v) {
  const u32 d = pk(v, dpp<SHL + 1>(v));
  u4 a; a[0] = d; a[1] = dppu<SHL + 2>(d); a[2] = dppu<SHL + 4>(d); a[3] = dppu<SHL + 6>(d);
  return a;
}

template <int I> struct IC { static constexpr int value = I; };
template <int I, int N, class F> DI void sfor(F&& f) { if constexpr (I < N) { f(IC<I>{}); sfor<I + 1, N>(f); } }

DI void swap16(u32 d, u32& r0, u32& r1) { auto r = __builtin_amdgcn_permlane16_swap(d, d, false, false); r0 = r[0]; r1 = r[1]; }

template <int O> DI void mload(u4& v, u4 rs, int off, u64 m) {
  asm volatile("s_mov_b64 exec, %2\n\tbuffer_load_dwordx4 %0, %1, %3, %4 offen offset:%5 sc1\n\ts_mov_b64 exec, -1" : "+v"(v) : "v"(off), "s"(m), "s"(rs), "s"(O & ~4095), "n"(O & 4095) : "memory");
}
DI u64 missing(u4& v, u32 tag) { PIN(v); const u32 t1 = v[1], t3 = v[3]; return __ballot(t1 != tag) | __ballot(t3 != tag); }
template <int N, int STR, int SL> DI void poll(u4* v, u4 rs, int off, u32 tag) {
  u64 m[N], any;
#pragma unroll
  for (int i = 0; i < N; ++i) { m[i] = ~0ull; asm volatile("" : "=v"(v[i])); }
  int n = SPIN_G;
#pragma nounroll
  do {
    sfor<0, N>([&](auto I) __attribute__((always_inline)) { mload<I.value * STR>(v[I.value], rs, off, m[I.value]); });
    VMCNT(0);
    any = 0;
#pragma unroll
    for (int i = 0; i < N; ++i) { m[i] = missing(v[i], tag); any |= m[i]; }
    if (--n < 0) any = 0;
    if (SL && any) __builtin_amdgcn_s_sleep(SL);
  } while (any);
}
typedef u32 u16v __attribute__((ext_vector_type(16)));
typedef u32 u8v __attribute__((ext_vector_type(8)));
struct Land { u16v a, b, c; };
struct Land2 { u8v a, b, c; };
#define PREG(d, i, e) "v[%[lb]+%[ls]*" #d "+4*" #i "+" #e "]"
#define PLD(d, i, o) "s_mov_b64 exec, %[m" #i "]\n\tbuffer_load_dwordx4 v[%[lb]+%[ls]*" #d "+4*" #i ":%[lb]+3+%[ls]*" #d "+4*" #i "], %[" #o "], %[rs], 0 offen offset:%[i" #i "] sc1\n\t"
#define PMG(d, i) "s_mov_b64 exec, %[m" #i "]\n\tv_cmpx_eq_u32_e32 vcc, %[tag], " PREG(d, i, 1) "\n\tv_cmpx_eq_u32_e32 vcc, %[tag], " PREG(d, i, 3) "\n\t" \
  "v_mov_b32 %[a" #i "], " PREG(d, i, 0) "\n\tv_mov_b32 %[b" #i "], " PREG(d, i, 2) "\n\ts_andn2_b64 %[m" #i "], %[m" #i "], exec\n\t"
#define PI4(d) PLD(d, 0, o) PLD(d, 1, o) PLD(d, 2, o) PLD(d, 3, p)
#define PM4(d) PMG(d, 0) PMG(d, 1) "s_or_b64 %[t], %[m0], %[m1]\n\t" PMG(d, 2) "s_or_b64 %[t], %[t], %[m2]\n\t" PMG(d, 3) "s_or_b64 %[t], %[t], %[m3]\n\t"
#define PSLOW ".if %[sl]>0\n3:\n\t" PI4(0) "s_waitcnt vmcnt(0)\n\t" PM4(0) "s_cbranch_scc0 2f\n\t" \
  "s_bcnt1_i32_b64 vcc_lo, %[m0]\n\ts_bcnt1_i32_b64 vcc_hi, %[m1]\n\ts_add_u32 vcc_lo, vcc_lo, vcc_hi\n\ts_bcnt1_i32_b64 vcc_hi, %[m2]\n\ts_add_u32 vcc_lo, vcc_lo, vcc_hi\n\t" \
  "s_bcnt1_i32_b64 vcc_hi, %[m3]\n\ts_add_u32 vcc_lo, vcc_lo, vcc_hi\n\ts_cmpk_le_u32 vcc_lo, %[thr]\n\ts_cbranch_scc1 4f\n\ts_sleep %[sl]\n\t" \
  "s_sub_u32 %[n], %[n], 1\n\ts_cbranch_scc0 3b\n\ts_branch 2f\n4:\n\t.endif\n\t"
#define PR4(d) "s_waitcnt vmcnt(%[w])\n\t" PM4(d) "s_cbranch_scc0 2f\n\t" PI4(d)
#define PI2(d) PLD(d, 0, o) PLD(d, 1, p)
#define PR2(d) "s_waitcnt vmcnt(%[w])\n\t" PMG(d, 0) PMG(d, 1) "s_or_b64 %[t], %[m0], %[m1]\n\ts_cbranch_scc0 2f\n\t" PI2(d)
#define PEND "s_sub_u32 %[n], %[n], 1\n\ts_cbranch_scc0 1b\n2:\n\ts_mov_b64 exec, -1"
#define PLOOP(I, R) I(0) ".if %[dp]>1\n\ts_sleep %[gap]\n\t" I(1) ".endif\n\t.if %[dp]>2\n\ts_sleep %[gap]\n\t" I(2) ".endif\n1:\n\t" \
  R(0) ".if %[dp]>1\n\t" R(1) ".endif\n\t.if %[dp]>2\n\t" R(2) ".endif\n\t" PEND
#define PLAND "=&{v[108:123]}"(l.a), "=&{v[124:139]}"(l.b), "=&{v[140:155]}"(l.c)
#define POUT4 [a0] "+v"(r[0]), [b0] "+v"(r[1]), [a1] "+v"(r[2]), [b1] "+v"(r[3]), [a2] "+v"(r[4]), [b2] "+v"(r[5]), [a3] "+v"(r[6]), [b3] "+v"(r[7]), \
  [m0] "+s"(m0), [m1] "+s"(m1), [m2] "+s"(m2), [m3] "+s"(m3), [t] "=&s"(t), [n] "+s"(n)
template <int DP, int GAP, int STR, int THR, int SL> DI void ppoll4(Land& l, u32* r, u4 rs, int o, u32 tag) {
  u64 m0 = ~0ull, m1 = ~0ull, m2 = ~0ull, m3 = ~0ull, t;
  u32 n = SPIN_G;
  asm volatile(PSLOW PLOOP(PI4, PR4) : PLAND, POUT4
    : [o] "v"(o), [p] "v"(o), [rs] "s"(rs), [tag] "s"(tag), [i0] "n"(0), [i1] "n"(STR), [i2] "n"(2 * STR), [i3] "n"(3 * STR), [w] "n"(4 * DP - 4), [dp] "n"(DP), [gap] "n"(GAP),
      [thr] "n"(THR), [sl] "n"(SL), [lb] "n"(108), [ls] "n"(16)
    : "memory", "vcc", "scc");
}
#define POUT2 [a0] "+v"(r[0]), [b0] "+v"(r[1]), [a1] "+v"(r[2]), [b1] "+v"(r[3]), [m0] "+s"(m0), [m1] "+s"(m1), [n] "+s"(n)
template <int DP, int GAP> DI void ppoll2(Land2& l, u32* r, u4 rs, int o, int p, u32 tag, u64 m0, u64 m1) {
  u64 t;
  u32 n = SPIN_G;
  asm volatile(PLOOP(PI2, PR2) : "=&{v[132:139]}"(l.a), "=&{v[140:147]}"(l.b), "=&{v[148:155]}"(l.c), POUT2, [t] "=&s"(t)
    : [o] "v"(o), [p] "v"(p), [rs] "s"(rs), [tag] "s"(tag), [i0] "n"(0), [i1] "n"(1024), [w] "n"(2 * DP - 2), [dp] "n"(DP), [gap] "n"(GAP), [lb] "n"(132), [ls] "n"(8)
    : "memory", "vcc", "scc");
}
template <int THR, int SL> DI void scout(u32* r, u4 rs, int o, int p, u32 tag, u64& m0, u64& m1) {
  u8v a;
  u32 n = SPIN_G;
  asm volatile("1:\n\t" PI2(0) "s_waitcnt vmcnt(0)\n\t" PMG(0, 0) PMG(0, 1) "s_bcnt1_i32_b64 vcc_lo, %[m0]\n\ts_bcnt1_i32_b64 vcc_hi, %[m1]\n\ts_add_u32 vcc_lo, vcc_lo, vcc_hi\n\t"
    "s_cmpk_le_u32 vcc_lo, %[thr]\n\ts_cbranch_scc1 3f\n\ts_sleep %[sl]\n\ts_sub_u32 %[n], %[n], 1\n\ts_cbranch_scc0 1b\n3:\n\ts_mov_b64 exec, -1"
    : "=&{v[132:139]}"(a), POUT2
    : [o] "v"(o), [p] "v"(p), [rs] "s"(rs), [tag] "s"(tag), [i0] "n"(0), [i1] "n"(1024), [thr] "n"(THR), [sl] "n"(SL), [lb] "n"(132), [ls] "n"(8)
    : "memory", "vcc", "scc");
}
DI void drain(const Land& l) { asm volatile("s_waitcnt vmcnt(0)" :: "{v[108:123]}"(l.a), "{v[124:139]}"(l.b), "{v[140:155]}"(l.c) : "memory"); }
DI void drain(const Land2& l) { asm volatile("s_waitcnt vmcnt(0)" :: "{v[132:139]}"(l.a), "{v[140:147]}"(l.b), "{v[148:155]}"(l.c) : "memory"); }
DI u4 cat(const u32* r) { return u4{r[0], r[1], r[2], r[3]}; }
DI f4 fold8(f4 d) {
  f4 r;
#pragma unroll
  for (int i = 0; i < 4; ++i) { const float t = d[i]; r[i] = t + dpp<ROR8>(t); }
  return r;
}
DI u4 selector(int lane) {
  const u32 one = (lane & 1) ? 0x3f800000u : 0x3f80u, a = (lane & 2) ? 0u : one, b = (lane & 2) ? one : 0u;
  return u4{a, b, a, b};
}
DI f4 last_sum(Land2& l, u4 rs, int r, int w, int lane, u32 tag) {
  u32 v[4] = {};
  u4 sel = selector(lane);
  int o = O_FC + (r * 144 + 16 * w) * 128 + lane * 16;
  PIN(sel); PIN(o);
  if (FDL) __builtin_amdgcn_s_sleep(FDL);
  ppoll2<DPF, GPF>(l, v, rs, o, o, tag, ~0ull, ~0ull);
  const f4 z = {0.f, 0.f, 0.f, 0.f};
  return mfma(sel, cat(v), z);
}

template <int W> DI void wo_load(const u16* wo, int b, int j, int lane, u4* F) {
  const rsrc_t r = mkrs(wo + (size_t)(4 * (b & 7) + j) * 128 * D, 128 * D * 2);
  const int o = (16 * (lane >> 4) + (lane & 8)) * (D * 2) + 128 * (b >> 3) + 16 * (lane & 7);
  u4 w[16];
#pragma unroll
  for (int i = 0; i < 16; ++i) { if (i >= W) VMCNT(W - 1); w[i] = __builtin_amdgcn_raw_buffer_load_b128(r, o, (64 * (i >> 3) + (i & 7)) * (D * 2), NT); SB(); }
  frag<4>(w, F);
  frag<4>(w + 8, F + 8);
}
#define Y_PREP(j) \
      int yo = O_YA + (((4 * (j) + (lane >> 4)) * 9 + (b >> 3)) * 8 + (b & 7)) * 256 + (lane & 15) * 16; \
      int po = O_OP + (32 * (b & 7) + 8 * (j) + ((lane & 15) >> 1)) * 512 + (2 * (lane >> 4) + (lane & 1)) * 16; \
      LAS float* yw = (lane & 8 ? &sh->un[j][0] : &sh->mp[j][0]) + 8 * (lane & 6) + ((lane >> 1) & 8) + 2 * (lane & 1) + (lane >> 5); \
      const LAS f4* yr = (const LAS f4*)&sh->mp[0][0] + (lane & 15); \
      u4 yv = {0u, tag, 0u, tag}; \
      PIN(yo); PIN(po); PIN(yw); PIN(yr); PIN(yv);
#define Y_TAIL() { \
      Land ps; \
      u32 v[8] = {}; \
      ppoll4<DPO, GPO, 128, 32 * OTH, YSL>(ps, v, rs, po, tag); \
      STAMP(10); \
      const u4 a0 = cat(v), a1 = cat(v + 4); \
      float y[8], t4[4]; \
      f4 d[8]; \
      _Pragma("unroll") for (int i = 0; i < 8; ++i) d[i] = mfma(a0, F[i], f4{0.f, 0.f, 0.f, 0.f}); \
      SB(); \
      _Pragma("unroll") for (int i = 0; i < 8; ++i) { d[i] = mfma(a1, F[8 + i], d[i]); SB(); } \
      f2 e[8]; \
      _Pragma("unroll") for (int i = 0; i < 8; ++i) { e[i] = d[i].xy + d[i].zw; SB(); } \
      _Pragma("unroll") for (int i = 0; i < 8; ++i) { const float e0 = e[i][0], e1 = e[i][1]; y[i] = e0 + dpp<ROR8>(e1); }     \
      SB(); \
      _Pragma("unroll") for (int i = 0; i < 4; ++i) t4[i] = sw32(y[i], y[i + 4]); \
      yw[0] = sw16(t4[0], t4[2]); yw[4] = sw16(t4[1], t4[3]); \
      bar();                                                             \
      const f4 r = (yr[0] + yr[16]) + (yr[32] + yr[48]); \
      yv[0] = pk(r[0], r[1]); yv[2] = pk(r[2], r[3]); \
      _Pragma("unroll") for (int i = 0; i < YR / 16; ++i) __builtin_amdgcn_raw_buffer_store_b128(yv, rws, yo, i * 16 * 9 * 2048, SC1); \
      STAMP(11); \
      drain(ps); }

DI void gu_pass(u4 a, u4 e, const u4* F, u32* r) {
  f4 d[4];
  float y[4];
  SB();
#pragma unroll
  for (int i = 0; i < 4; ++i) d[i] = mfma(a, F[i], f4{0.f, 0.f, 0.f, 0.f});
  SB();
#pragma unroll
  for (int i = 0; i < 4; ++i) d[i] = mfma(e, F[4 + i], d[i]);
  SB();
#pragma unroll
  for (int i = 0; i < 4; ++i) {
    const f2 t = d[i].xy + d[i].zw;
    const float t0 = t[0], t1 = t[1];
    y[i] = t0 + dpp<ROR8>(t1);
  }
#pragma unroll
  for (int i = 0; i < 2; ++i) { auto t = __builtin_amdgcn_permlane16_swap(asu(y[i]), asu(y[i + 2]), false, false); r[2 * i] = t[0]; r[2 * i + 1] = t[1]; }
}
constexpr int win(int t) { return PS > 0 && t >= P0 ? WV : WK; }
constexpr int due(int t) {
  constexpr int kt[] = {3, 6, 10, 16, 24, 32, 40, 48, 56, 64}, kv[] = {PKNOTS};
  int i = 0;
  while (kt[i + 1] <= t) ++i;
  return POFF + (kv[i] + (kv[i + 1] - kv[i]) * (t - kt[i]) / (kt[i + 1] - kt[i])) * PS / 1000;
}
constexpr int QS = 32 + PV0 - 1;
constexpr int pv_done(int t) {
  int n = 0;
  for (int i = QS + 5; i <= t; ++i) { const int av = i - 32 - win(i) + 1; n = n + 3 < av ? n + 3 : (av > n ? av : n); }
  return n;
}

__global__ void __launch_bounds__(768, 3)
fused_kernel(const u16* x, const u16* kc, const u16* vc, const u16* wq, const u16* wo, const u16* wg, const u16* wu, const u16* wd,
             const u16* cv, const u16* wmod, u16* out, char* ws) {
  __shared__ Sh shm;
  LAS Sh* sh = (LAS Sh*)&shm;
  const int lane = threadIdx.x & 63, wave = __builtin_amdgcn_readfirstlane(threadIdx.x >> 6);
  const int bx = blockIdx.x, xc = bx & 7, ci = bx >> 3, s = ci & 7, h = (ci >> 3) * 8 + xc, pi = 8 * h + s, b = (bx + RO) & 255;
  u32 cpf = 0;
  if (CPF) cpf = __builtin_amdgcn_raw_buffer_load_b32(mkrs((const void*)(__builtin_amdgcn_s_getpc() & ~255ull), CPF), (((ci & 3) * 64 + lane) * 128) | (wave == 11 ? 0 : 1 << 30), 0, 0);
  const u32 tz = (u32)__builtin_amdgcn_s_memrealtime();
  SB();
  const size_t slab = ((size_t)h * 8192 + s * 1024) * 128;
  const int vo = (wave >> 2) * 131072 + (wave & 3) * 1024 + lane * 16;
  asm volatile("" : "+s"(x), "+s"(kc), "+s"(vc), "+s"(wq), "+s"(wo), "+s"(wg), "+s"(wu), "+s"(wd), "+s"(cv), "+s"(wmod), "+s"(out), "+s"(ws));
  const rsrc_t rk = mkrs(kc + slab, 262144);
  u4 kb[32];
#pragma unroll
  for (int t = 0; t < WK; ++t) kb[t] = __builtin_amdgcn_raw_buffer_load_b128(rk, vo, t * 4096, NT);
  SB();
  if (threadIdx.x < 16) sh->flag[threadIdx.x] = 0;
  bar();
  const int fr = b - NY, cb = ((b - S1) * 57) >> 9, rk_ = b < FE ? fr >> 1 : b < S1 ? 0 : b - S1 - 9 * cb;
  const rsrc_t rws = mkrs(ws, O_END);
  const u4 rs = {(u32)(size_t)ws, (u32)((size_t)ws >> 32) & 0xffffu, O_END, 0x00020000u};

  if (__builtin_expect(wave >= 8, 1)) {
#define KEEP() { asm volatile("" :: "v"(cpf) : "memory"); _Pragma("unroll") for (int t = 0; t < WK; ++t) asm volatile("" :: "v"(kb[t]) : "memory"); SB(); }
    if (__builtin_expect(wave < 11, 1)) {
      const int qt = bx < 72 ? (bx * 57) >> 10 : ((bx - 72) * 57) >> 11, ln = bx < 72 ? bx - 18 * qt : bx - 54 - 36 * qt;
      const int j = wave - 8, r0 = 144 * qt + 48 * j + 6 * (lane >> 3);
      const rsrc_t rc = mkrs(cv, D * 2), rm = mkrs(wmod, bx < 216 ? D * 6 * D * 2 : 0);
      const int mo = r0 * (6 * D * 2) + (64 * ln + 8 * (lane & 7)) * 2;
      u4 wm[6];
#pragma unroll
      for (int i = 0; i < 6; ++i) wm[i] = __builtin_amdgcn_raw_buffer_load_b128(rm, mo, i * (6 * D * 2), NT);
      const u3 cr = __builtin_amdgcn_raw_buffer_load_b96(rc, r0 * 2, 0, 0);
      const u32 ep = __builtin_amdgcn_raw_buffer_load_b32(rws, O_EPOCH, 0, SC1);
      SB();
      KEEP();
      float a[8];
#pragma unroll
      for (int e = 0; e < 8; ++e) a[e] = 0.f;
      u32 P[3][8];
#pragma unroll
      for (int p = 0; p < 3; ++p)
#pragma unroll
        for (int e = 0; e < 4; ++e) { const u32 u = wm[2 * p][e], v = wm[2 * p + 1][e]; P[p][2 * e] = plo(u, v); P[p][2 * e + 1] = phi(u, v); }
#pragma unroll
      for (int p = 0; p < 3; ++p) {
        const u32 w = cr[p], act = pk(silu(lo16(w)), silu(hi16(w)));
#pragma unroll
        for (int e = 0; e < 8; ++e) a[e] = dot2(P[p][e], act, a[e]);
      }
      float t[4], u[2];
#pragma unroll
      for (int i = 0; i < 4; ++i) t[i] = sw32(a[i], a[i + 4]);
#pragma unroll
      for (int i = 0; i < 2; ++i) u[i] = sw16(t[i], t[i + 2]);
      const bool b3 = lane & 8;
      sh->mp[j][8 * (lane & 7) + 2 * (lane >> 4) + ((lane >> 3) & 1)] = (b3 ? u[1] : u[0]) + dpp<ROR8>(b3 ? u[0] : u[1]);
      lds_set(&sh->flag[j], lane);
      const u32 tag = __builtin_amdgcn_readfirstlane(((ep | 0x40004000u) + 1) & 0xbfffbfffu);
      STAMP(1);
      if (wave == 8 && bx < 216) {
        lds_wait<1>(&sh->flag[1]); lds_wait<1>(&sh->flag[2]);
        const int l = 2 * (lane & 31);
        const f2 m = *(const LAS f2*)&sh->mp[0][l] + *(const LAS f2*)&sh->mp[1][l] + *(const LAS f2*)&sh->mp[2][l];
        const float m0 = m[0], m1 = m[1];
        const u4 o = {asu(m0), tag, asu(m1), tag};
        if (lane < 32) st128(o, rws, O_MOD + qt * MOD_STR + ln * 512 + lane * 16);
        STAMP(2);
      }
      if (b < NY) {
        u4 F[16];
        if (HQ & 1) lds_wait<16>(&sh->flag[3]);
        wo_load<2 * HW>(wo, b, j, lane, F);
        Y_PREP(j)
        STAMP(5);
        bar();
        STAMP(8);
        Y_TAIL()
      } else if (b >= S2) {
        const int f = b - S2;
        const rsrc_t rwd = mkrs(wd + (size_t)32 * f * D, 32 * D * 2);
        const int o = 8 * (lane >> 4) * (D * 2) + (192 * j + 4 * (lane & 15)) * 2;
        u2 w[3][8];
        u4 F[12];
        if (HQ & 2) lds_wait<16>(&sh->flag[3]);
#pragma unroll
        for (int i = 0; i < 8; ++i) {
          if (i >= WDW) VMCNT(3 * WDW - 3);
#pragma unroll
          for (int e = 0; e < 3; ++e) w[e][i] = __builtin_amdgcn_raw_buffer_load_b64(rwd, o + e * 128, i * (D * 2), NT);
          SB();
        }
#pragma unroll
        for (int e = 0; e < 3; ++e) frag<2>(w[e], F + 4 * e);
        int so = O_S + f * S_STR + j * 1536 + lane * 16, to = lane < 32 ? so : O_S + f * S_STR + 4608 - 1024 - 512 + lane * 16, ao = so + (O_AB - O_S);
        int fo = lane < 16 ? O_FC + ((6 * j + (lane >> 3)) * 144 + f) * 128 + (lane & 7) * 16 : O_END;
        LAS float* gw = &sh->mp[j][2 * (lane & 31) + (lane >> 5)];
        const LAS float* gr = &sh->mp[0][4 * (lane & 8) + 8 * (lane >> 4) + (lane & 7)];
        Land2 ps;
        u32 v[4] = {}, c[4] = {};
        u64 m0 = ~0ull, m1 = ~0u;
        u4 o4[3];
#pragma unroll
        for (int k = 0; k < 3; ++k) { o4[k] = u4{0u, tag, 0u, tag}; PIN(o4[k]); }
        PIN(so); PIN(to); PIN(ao); PIN(fo); PIN(gw); PIN(gr);
        STAMP(5);
        bar(); bar();
        STAMP(8);
        bar();
        STAMP(10);
        scout<0, 0>(c, rs, ao, ao, tag, m0, m1);
        gw[256] = sw32(lo16(c[0]) + lo16(c[2]), lo16(c[1]) + lo16(c[3]));
        gw[448] = sw32(hi16(c[0]) + hi16(c[2]), hi16(c[1]) + hi16(c[3]));
        STAMP(9);
        if (SDL) __builtin_amdgcn_s_sleep(SDL);
        ppoll2<DPS, GPS>(ps, v, rs, so, to, tag, ~0ull, (1ull << 41) - 1);
        STAMP(11);
        const auto t = __builtin_amdgcn_permlane32_swap(v[2], v[3], false, false);
        const u32 t0 = t[0], t1 = t[1];
        *gw = sw32(asf(v[0]), asf(v[1])) + asf(t0);
        SB();
        float st = row_sum(asf(t1)) * (1.f / D);
        PIN(st);
        bar();
        STAMP(12);
        float e[9];
#pragma unroll
        for (int i = 0; i < 9; ++i) e[i] = gr[64 * (i + (i > 2))];
        SB();
        const float mean = asf(__builtin_amdgcn_readlane(asu(st), 0)), e2 = asf(__builtin_amdgcn_readlane(asu(st), 32));
        float rsd = __builtin_amdgcn_rsqf(fmaxf(fmaf(-mean, mean, e2), 0.f) + 1e-5f);
        PIN(rsd);
        SB();
        const float gs = fmaf(fmaf(-mean, (e[3] + e[4]) + e[5], (e[0] + e[1]) + e[2]), rsd, (e[6] + e[7]) + e[8]);
        const float us = dpp<SHL + 8>(gs);
        const u4 a4 = gather8(silu(gs) * us);
        f4 d[12];
        SB();
#pragma unroll
        for (int i = 0; i < 12; ++i) { d[i] = mfma(a4, F[i], f4{0.f, 0.f, 0.f, 0.f}); SB(); }
#pragma unroll
        for (int k = 0; k < 3; ++k) {
#pragma unroll
          for (int n = 0; n < 4; ++n) PIN(d[4 * k + n]);
          o4[k][0] = pk(d[4 * k][0], d[4 * k + 1][0]); o4[k][2] = pk(d[4 * k + 2][0], d[4 * k + 3][0]);
          __builtin_amdgcn_raw_buffer_store_b128(o4[k], rws, fo, k * 2 * 144 * 128, SC1);
          SB();
        }
        STAMP(13);
        drain(ps);
      }
    } else {
      const rsrc_t rx = mkrs(x, D * 2), rq = mkrs(wq, D * 4096 * 2);
      const int g = lane >> 4, pc = (lane & 15) < 9 ? (lane & 15) : 8, pidx = 36 * s + 9 * g + pc;
      const u4 xa = __builtin_amdgcn_raw_buffer_load_b128(rx, lane * 16, 0, 0);
      const u4 xb = __builtin_amdgcn_raw_buffer_load_b128(rx, 1024 + lane * 16, 0, 0);
      const u32 xp = __builtin_amdgcn_raw_buffer_load_b32(rx, pidx * 4, 0, 0);
      const u32 ep = __builtin_amdgcn_raw_buffer_load_b32(rws, O_EPOCH, 0, SC1);
      u4 wv[18];
      const int qo = (72 * s + 18 * g) * 8192 + (128 * h + 8 * (lane & 15)) * 2;
#pragma unroll
      for (int i = 0; i < 18; ++i) { wv[i] = __builtin_amdgcn_raw_buffer_load_b128(rq, qo, i * 8192, NT); SB(); }
      KEEP();
      const u32 tag = __builtin_amdgcn_readfirstlane(((ep | 0x40004000u) + 1) & 0xbfffbfffu);
      sh->tag = tag;
      float l0, l1;
      {
        float sum = 0.f;
#pragma unroll
        for (int i = 0; i < 4; ++i) { const u32 u = xa[i], v = xb[i]; sum += (lo16(u) + hi16(u)) + (lo16(v) + hi16(v)); }
        const float mean = wave_sum(sum) * (1.f / D);
        float sa = 0.f, sb = 0.f;
#pragma unroll
        for (int i = 0; i < 4; ++i) {
          const u32 u = xa[i], v = xb[i];
          const float d0 = lo16(u) - mean, d1 = hi16(u) - mean, d2 = lo16(v) - mean, d3 = hi16(v) - mean;
          sa = fmaf(d0, d0, fmaf(d1, d1, sa)); sb = fmaf(d2, d2, fmaf(d3, d3, sb));
        }
        const float rs = ln_rstd(wave_sum(lane < 8 ? sa + sb : sa) * (1.f / D));
        l0 = rb(rb(lo16(xp) - rb(mean)) * rs); l1 = rb(rb(hi16(xp) - rb(mean)) * rs);
        PIN(l0); PIN(l1);
      }
      u32 P[9][8];
#pragma unroll
      for (int p = 0; p < 9; ++p)
#pragma unroll
        for (int e = 0; e < 4; ++e) { const u32 u = wv[2 * p][e], v = wv[2 * p + 1][e]; P[p][2 * e] = plo(u, v); P[p][2 * e + 1] = phi(u, v); PIN(P[p][2 * e]); PIN(P[p][2 * e + 1]); }
      STAMP(1);
      {
        int o = O_MOD + pidx * 16;
        u4 v[8];
        until(tz, TP1);
#pragma nounroll
        for (u32 n = 0; n < SPIN_G; ++n) {
          FENCE(); PIN(o);
          u32 bad = 0;
#pragma unroll
          for (int i = 0; i < 8; ++i) v[i] = ld128(rws, o + (i >> 1) * MOD_STR + (i & 1) * 4608);
#pragma unroll
          for (int i = 0; i < 8; ++i) bad |= (v[i][1] ^ tag) | (v[i][3] ^ tag);
          if (__all(bad == 0)) break;
          __builtin_amdgcn_s_sleep(PSL);
        }
        STAMP(2);
        float m[4];
#pragma unroll
        for (int i = 0; i < 4; ++i) {
          const u32 e0 = v[i >> 1][2 * (i & 1)], e1 = v[2 + (i >> 1)][2 * (i & 1)], e2 = v[4 + (i >> 1)][2 * (i & 1)], e3 = v[6 + (i >> 1)][2 * (i & 1)];
          m[i] = rb((asf(e0) + asf(e1)) + (asf(e2) + asf(e3)));
        }
        const u32 hp = pk(rb(l0 * rb(1.f + m[2])) + m[0], rb(l1 * rb(1.f + m[3])) + m[1]);
        float a[8];
#pragma unroll
        for (int e = 0; e < 8; ++e) a[e] = 0.f;
#pragma unroll
        for (int p = 0; p < 9; ++p) {
          const u32 hb = nbc(hp, p);
#pragma unroll
          for (int e = 0; e < 8; ++e) a[e] = dot2(P[p][e], hb, a[e]);
        }
        float t[4];
#pragma unroll
        for (int i = 0; i < 4; ++i) t[i] = sw32(a[i], a[i + 4]);
        const u4 qo4 = {asu(sw16(t[0], t[2])), tag, asu(sw16(t[1], t[3])), tag};
        st128(qo4, rws, O_QP + pi * 1024 + lane * 16);
      }
      {
        int o = O_QP + h * 8192 + lane * 16;
        u4 v[8];
        until(tz, TP2);
#pragma nounroll
        for (u32 n = 0; n < SPIN_G; ++n) {
          FENCE(); PIN(o);
          u32 bad = 0;
#pragma unroll
          for (int i = 0; i < 8; ++i) v[i] = ld128(rws, o + i * 1024);
#pragma unroll
          for (int i = 0; i < 8; ++i) bad |= (v[i][1] ^ tag) | (v[i][3] ^ tag);
          if (__all(bad == 0)) break;
          __builtin_amdgcn_s_sleep(PSL);
        }
        float q0 = 0.f, q1 = 0.f;
#pragma unroll
        for (int i = 0; i < 8; ++i) { const u32 u = v[i][0], w = v[i][2]; q0 += asf(u); q1 += asf(w); }
        sh->q[4 * (lane & 15) + g] = pk(q0, q1);
        lds_set(&sh->flag[3], lane);
      }
      STAMP(3);
      if (b >= NY && b < FE) {
        const int pr = 16 * fr + (lane & 15);
        u4 v[4];
        poll<4, MOD_STR, PSL>(v, rs, O_MOD + (2 + g) * 4608 + pr * 16, tag);
        float m[2];
#pragma unroll
        for (int i = 0; i < 2; ++i) {
          const u32 e0 = v[0][2 * i], e1 = v[1][2 * i], e2 = v[2][2 * i], e3 = v[3][2 * i];
          m[i] = rb((asf(e0) + asf(e1)) + (asf(e2) + asf(e3))) + (g == 2 ? 1.f : 0.f);
        }
        __builtin_amdgcn_raw_buffer_store_b64(u2{pk(m[0], m[1]), tag}, rws, O_MC + (288 * g + pr) * 8, 0, SC1);
      }
      float M;
      {
#pragma nounroll
        for (u32 n = 0; n < SPIN_L; ++n) {
          if (__all(((volatile LAS u32*)sh->flag)[8 + (lane & 7)] != 0)) break;
          __builtin_amdgcn_s_sleep(4);
        }
        FENCE();
        const f2 m = sh->ml[lane & 7];
        M = max8(m[0]);
        const float L = sum8(m[1] * __expf(m[0] - M));
        const u4 o = {asu(M), tag, asu(L), tag};
        if (lane == 0) st128(o, rws, O_ML + pi * 16);
        sh->gm = M;
        lds_set(&sh->flag[4], lane);
      }
      STAMP(4);
#define O_PART() { \
        STAMP(5); \
        lds_wait<8>(&sh->flag[6]); \
        u4 m; \
        poll<1, 0, PSL>(&m, rs, O_ML + (8 * h + (lane & 7)) * 16, tag); \
        const u32 mu = m[0], lu = m[2]; \
        const float Mh = max8(asf(mu)); \
        float cw = __expf(M - Mh) * __builtin_amdgcn_rcpf(sum8(asf(lu) * __expf(asf(mu) - Mh))); \
        const LAS f2* op = &sh->ob[0][((lane >> 1) & 7) + 8 * (lane >> 5) + 16 * (lane & 1) + 40 * ((lane >> 4) & 1)]; \
        int oo = O_OP + pi * 512 + lane * 8; \
        u2 ov = {0u, tag}; \
        PIN(op); PIN(oo); PIN(cw); PIN(ov); \
        STAMP(7); \
        bar();                                                           \
        STAMP(8); \
        f2 t[8]; \
        _Pragma("unroll") for (int i = 0; i < 8; ++i) t[i] = op[72 * i]; \
        const f2 r = (((t[0] + t[1]) + (t[2] + t[3])) + ((t[4] + t[5]) + (t[6] + t[7]))) * cw; \
        const float r0 = r[0], r1 = r[1]; \
        ov[0] = pk(r0, r1); \
        __builtin_amdgcn_raw_buffer_store_b64(ov, rws, oo, 0, SC1); }
      if (b < NY) {
        u4 F[16];
        wo_load<CW>(wo, b, 3, lane, F);
        Y_PREP(3)
        O_PART()
        Y_TAIL()
      } else {
        float ga[4], xi[4];
        f2 c1[2];
        {
          const u2 e = __builtin_amdgcn_raw_buffer_load_b64(rx, 128 * rk_ + 32 * g + 8 * (lane & 3), 0, 0);
          u4 m;
          poll<1, 0, PSL>(&m, rs, O_MC + (288 * g + 32 * rk_) * 8 + (lane & 15) * 16, tag);
          const u32 m0 = m[0], m1 = m[2];
          ((LAS u2*)sh->mc[0])[lane] = u2{m0, m1};
          LDSW();
          const u2 a = *(const LAS u2*)&sh->mc[0][8 * g + 2 * (lane & 3)], c = *(const LAS u2*)&sh->mc[2][8 * g + 2 * (lane & 3)];
#pragma unroll
          for (int i = 0; i < 2; ++i) {
            const u32 a_ = a[i], e_ = e[i], c_ = c[i];
            ga[2 * i] = lo16(a_); ga[2 * i + 1] = hi16(a_); xi[2 * i] = lo16(e_); xi[2 * i + 1] = hi16(e_); c1[i] = f2{lo16(c_), hi16(c_)}; PIN(c1[i]);
          }
#pragma unroll
          for (int i = 0; i < 4; ++i) { PIN(ga[i]); PIN(xi[i]); }
        }
        u4 sel = selector(lane);
        u32 msk = (lane & 4) ? 0xffff0000u : 0u, one = 0x3f803f80u;
        int yo = O_YA + (((b < S1 ? 2 + (b & 1) : cb) % YR) * 9 + rk_) * 2048 + lane * 16;
        int to = b >= S1 && lane < 8 ? O_S + (8 * cb + lane) * S_STR + 4608 + rk_ * 16 : O_END;
        LAS u2* zw = (LAS u2*)&sh->zz[(lane >> 3) & 1][lane] + ((lane >> 1) & 1);
        sh->zz[0][lane] = u4{0u, 0u, 0u, 0u}; sh->zz[1][lane] = u4{0u, 0u, 0u, 0u};
        PIN(sel); PIN(msk); PIN(one); PIN(yo); PIN(to); PIN(zw);
        O_PART()
        if (b >= S1) bar();
        if (b >= S1 || b < FE) {
        STAMP(9);
        Land2 ps;
        u32 v[4] = {};
        u64 m0 = ~0ull, m1 = ~0ull;
        if (YDL) __builtin_amdgcn_s_sleep(YDL);
        scout<16 * ATH, ASL>(v, rs, yo, yo, tag, m0, m1);
        ppoll2<DPY, GPY>(ps, v, rs, yo, yo, tag, m0, m1);
        STAMP(10);
        const f4 y = mfma(cat(v), sel, f4{0.f, 0.f, 0.f, 0.f});
        const u32 d0 = pk(fmaf(ga[0], y[0], xi[0]), fmaf(ga[1], y[1], xi[1])), d1 = pk(fmaf(ga[2], y[2], xi[2]), fmaf(ga[3], y[3], xi[3]));
        f2 z0 = f2{lo16(d0), hi16(d0)} * c1[0], z1 = f2{lo16(d1), hi16(d1)} * c1[1];
        PIN(z0); PIN(z1);
        z0 -= __builtin_bit_cast(f2, __builtin_bit_cast(u2, z0) & msk);
        z1 -= __builtin_bit_cast(f2, __builtin_bit_cast(u2, z1) & msk);
        *zw = u2{phi(asu(z0.x), asu(z0.y)), phi(asu(z1.x), asu(z1.y))};
        SB();
        if (STB) bar();
        float s = sw32(dot2(d0, one, dot2(d1, one, 0.f)), dot2(d0, d0, dot2(d1, d1, 0.f)));
        s += dpp<QX1>(s); s += dpp<QX2>(s);
        s = sw16(s, s);
        const auto t = __builtin_amdgcn_permlane32_swap(asu(s), asu(s), false, false);
        const u32 t0 = t[0], t1 = t[1];
        st128(u4{t0, tag, t1, tag}, rws, to);
        if (!STB) bar();
        STAMP(11);
        drain(ps);
        if (b < FE) {
          if (fr == 0 && lane == 0) __builtin_amdgcn_raw_buffer_store_b32(tag, rws, O_EPOCH, 0, SC1);
          const int src = 128 * (fr & 1) + 16 * (lane & 4) + 4 * (lane & 3);
          const u32 x0 = __builtin_amdgcn_ds_bpermute(src, d0), x1 = __builtin_amdgcn_ds_bpermute(src, d1);
          const u2 gb = *(const LAS u2*)&sh->mc[3][16 * (fr & 1) + 2 * (lane & 7)];
          const u32 gb0 = gb[0], gb1 = gb[1];
          float e[8] = {lo16(gb0), hi16(gb0), lo16(gb1), hi16(gb1), lo16(x0), hi16(x0), lo16(x1), hi16(x1)};
#pragma unroll
          for (int i = 0; i < 8; ++i) PIN(e[i]);
          Land2 fs;
          const LAS f4* p = (const LAS f4*)&sh->ob[0][0] + 4 * (lane & 8) + (lane & 7);
          PIN(p);
          const f4 d = last_sum(fs, rs, fr, 8, lane, tag);
          STAMP(12);
          bar();
          const f4 r = fold8(((p[0] + p[8]) + (p[16] + p[24])) + d);
          const u2 o = {pk(fmaf(e[0], r[0], e[4]), fmaf(e[1], r[1], e[5])), pk(fmaf(e[2], r[2], e[6]), fmaf(e[3], r[3], e[7]))};
          __builtin_amdgcn_raw_buffer_store_b64(o, mkrs(out + 32 * fr, 64), lane * 8, 0, 0);
          STAMP(13);
          drain(fs);
        }
        }
      }
    }
  } else {
    const rsrc_t rv = mkrs(vc + slab, 262144);
    u4 vb[32], qv;
    float sp[32], s0 = 0.f, mw = 0.f, p0 = 0.f, p1 = 0.f, acc[8];
#pragma unroll
    for (int i = 0; i < 8; ++i) acc[i] = 0.f;
#define QK(i) do { float a_ = dot2(kb[i][0], qv[0], 0.f), b_ = dot2(kb[i][1], qv[1], 0.f); a_ = dot2(kb[i][2], qv[2], a_); b_ = dot2(kb[i][3], qv[3], b_); sp[i] = a_ + b_; PIN(sp[i]); } while (0)
#define PV(i) do { const u32 pb_ = nbc(asu((i) < 16 ? p0 : p1), (i) & 15), pl_ = pb_ >> 16; \
      _Pragma("unroll") for (int cc = 0; cc < 4; ++cc) { const u32 w_ = vb[i][cc]; acc[2 * cc] = dot2(w_, pl_, acc[2 * cc]); acc[2 * cc + 1] = dot2(w_, pb_, acc[2 * cc + 1]); } \
      _Pragma("unroll") for (int cc = 0; cc < 8; ++cc) PIN(acc[cc]); } while (0)
    sfor<WK, 64>([&](auto T) __attribute__((always_inline)) {
      constexpr int t = T.value;
      if constexpr (PS > 0 && t >= P0) {
        asm volatile("s_memrealtime vcc\n\ts_waitcnt vmcnt(%1) lgkmcnt(0)\n\ts_sub_u32 vcc_lo, vcc_lo, %0\n\ts_cmpk_ge_u32 vcc_lo, %2\n\ts_cbranch_scc1 1f\n\ts_waitcnt vmcnt(%3)\n1:"
                     :: "s"(tz), "n"(WV - 1), "n"(due(t)), "n"(WK - 1) : "memory", "vcc", "scc");
      } else {
        VMCNT(win(t) - 1);
      }
      if constexpr (t == WK) { if (CPF) asm volatile("" ::"v"(cpf)); STAMP(1); }
      if constexpr (t == 32) STAMP(4);
      if constexpr (t < 32) kb[t] = __builtin_amdgcn_raw_buffer_load_b128(rk, vo, t * 4096, NT);
      else vb[t - 32] = __builtin_amdgcn_raw_buffer_load_b128(rv, vo, (t - 32) * 4096, NT);
      SB();
      if constexpr (t == QS) {
        STAMP(2);
        lds_wait<1>(&sh->flag[3]);
        STAMP(3);
        qv = *(const LAS u4*)(sh->q + 4 * (lane & 15));
      }
      if constexpr (t >= QS && t < QS + 4) {
#pragma unroll
        for (int j = 8 * (t - QS); j < 8 * (t - QS) + 8; ++j) QK(j);
        if constexpr (t == QS + 1) { s0 = tred16(sp, lane); PIN(s0); }
      }
      if constexpr (t == QS + 4) {
        float s1 = tred16(sp + 16, lane);
        s0 = rb(rb(s0) * 0.08838834764831845f);
        s1 = rb(rb(s1) * 0.08838834764831845f);
        mw = wave_max(fmaxf(s0, s1));
        p0 = rb(__expf(s0 - mw)); p1 = rb(__expf(s1 - mw));
        const float lw = wave_sum(p0 + p1);
        const f2 w = {mw, lw};
        sh->ml[wave] = w;
        LDSW();
        *(volatile LAS u32*)&sh->flag[8 + wave] = 1;
        STAMP(5);
      }
      if constexpr (t > QS + 4) {
        constexpr int j0 = pv_done(t - 1), j1 = pv_done(t);
#pragma unroll
        for (int j = j0; j < j1; ++j) PV(j);
      }
      if constexpr (t == LATE) *(volatile LAS u32*)&sh->flag[6] = 1;
      if constexpr (t == 48) STAMP(6);
      SB();
    });
    const rsrc_t rg = mkrs((wave < 4 ? wg : wu) + (b >= S1 ? 64 * (rk_ * FF + 4 * cb + (wave & 3)) : 0), b >= S1 ? 63 * FF * 2 + 128 : 0);
    const int go = ((lane & 48) + ((lane >> 1) & 4)) * (FF * 2) + 16 * (lane & 7);
    u4 G[8];
#define G_LOAD(i) G[i] = __builtin_amdgcn_raw_buffer_load_b128(rg, go, (2 * ((i) & 4) + ((i) & 3)) * FF * 2, NT)
    sfor<0, GN>([&](auto I) __attribute__((always_inline)) { G_LOAD(I.value); });
    SB();
    lds_wait<1>(&sh->flag[4]);
    float fw = __expf(mw - sh->gm);
    const u32 tag = __builtin_amdgcn_readfirstlane(sh->tag);
    LAS f2* ow = &sh->ob[wave][lane + 8 * (lane >> 5)];
    PIN(fw); PIN(ow);
    constexpr int PD = pv_done(63);
#pragma unroll
    for (int j = PD; j < 32; ++j) { PV(j); SB(); }
    float t4[4];
#pragma unroll
    for (int i = 0; i < 4; ++i) t4[i] = sw32(acc[i], acc[i + 4]);
    const f2 o2 = {sw16(t4[0], t4[2]) * fw, sw16(t4[1], t4[3]) * fw};
    *ow = o2;
    STAMP(7);
    bar();
    STAMP(8);
    if (b >= S1) {
      bar();
      sfor<GN, 8>([&](auto I) __attribute__((always_inline)) { G_LOAD(I.value); });
      SB();
      u4 F[8], o4 = {0u, tag, 0u, tag};
      int so = (lane & 8) ? O_END : O_S + (8 * cb + 2 * (wave & 3) + ((lane >> 2) & 1)) * S_STR + rk_ * 512 + (wave >> 2) * 256 + (lane & 3) * 64 + (lane & 48);
      const LAS u4* zr = &sh->zz[0][lane];
      const u2 p = *(const LAS u2*)&sh->mc[(lane & 4) ? 1 : 2][8 * (lane >> 4) + 2 * (lane & 3)];
      const u32 p0 = p[0], p1 = p[1], k = lane & 10;
      u4 a = {k == 0 ? p0 : 0u, k == 0 ? p1 : 0u, k == 2 ? p0 : 0u, k == 2 ? p1 : 0u}, e = {k == 8 ? p0 : 0u, k == 8 ? p1 : 0u, k == 10 ? p0 : 0u, k == 10 ? p1 : 0u};
      PIN(so); PIN(zr); PIN(a); PIN(e); PIN(o4);
      frag<4>(G, F);
      STAMP(10);
      u32 r[4];
      gu_pass(a, e, F, r);
      __builtin_amdgcn_raw_buffer_store_b128(u4{pk(asf(r[0]), asf(r[1])), tag, pk(asf(r[2]), asf(r[3])), tag}, rws, so, O_AB - O_S, SC1);
      STAMP(11);
      bar();
      STAMP(12);
      gu_pass(zr[0], zr[64], F, r);
      o4[0] = asu(asf(r[0]) + asf(r[1])); SB(); o4[2] = asu(asf(r[2]) + asf(r[3]));
      st128(o4, rws, so);
      STAMP(13);
    } else {
#pragma unroll
      for (int i = 0; i < GN; ++i) asm volatile("" ::"v"(G[i][0]));
      if (b >= NY && b < FE) {
        Land2 fs;
        LAS f4* p = (LAS f4*)&sh->ob[0][0] + 8 * wave + (lane & 7);
        PIN(p);
        bar();
        *p = fold8(last_sum(fs, rs, fr, wave, lane, tag));
        STAMP(14);
        bar();
        drain(fs);
      }
    }
  }
  STAMP(15);
  STAMP_OUT();
}

extern "C" void kernel_launch(void* const* d_in, const int* in_sizes, int n_in, void* d_out, int out_size, void* d_ws, size_t ws_size,
                              hipStream_t stream) {
  fused_kernel<<<256, 768, 0, stream>>>((const u16*)d_in[0], (const u16*)d_in[1], (const u16*)d_in[2], (const u16*)d_in[3],
                                        (const u16*)d_in[4], (const u16*)d_in[6], (const u16*)d_in[7], (const u16*)d_in[8],
                                        (const u16*)d_in[9], (const u16*)d_in[10], (u16*)d_out, (char*)d_ws);
}
```
